```python
import torch

def _softmax(x, axis=-1):
    return torch.softmax(x, dim=axis)

def _gelu(x):
    return torch.nn.functional.gelu(x, approximate='tanh')

def _silu(x):
    return torch.nn.functional.silu(x)

def _sigmoid(x):
    return torch.sigmoid(x)

def _layernorm(x, axis=-1, eps=1e-05):
    m = x.mean(dim=axis, keepdim=True)
    v = x.var(dim=axis, keepdim=True, unbiased=False)
    return (x - m) / torch.sqrt(v + eps)

def _scatter_add(tgt, idx, src, axis=0):
    return tgt.clone().index_add_(axis, idx, src)

def _segment_sum(src, seg, n):
    out = torch.zeros((n,) + src.shape[1:], dtype=src.dtype)
    return out.index_add_(0, seg, src)

def _pad(x, pw):
    flat = tuple((v for pair in reversed(pw) for v in pair))
    return torch.nn.functional.pad(x, flat)
kernel_name = 'adaLN_cond__sliding_block_cached__mixer_token_mlp__decode'

def setup_inputs():

    _w = lambda a: (lambda t: t.to(torch.bfloat16) if t.is_floating_point() else t)(torch.as_tensor(a))

    def _A_setup():
        B, Sq, D = (1, 1, 576)
        Hq, Hkv, Sk, Dh = (32, 16, 8192, 128)
        assert Hq % Hkv == 0, (Hq, Hkv)
        return {'x': _w((lambda *s: torch.randn(*s, dtype=torch.bfloat16) * 0.05)(B, Sq, D)), 'k_cache': _w((lambda *s: torch.randn(*s, dtype=torch.bfloat16) * 1.6)(B, Hkv, Sk, Dh)), 'v_cache': _w((lambda *s: torch.randn(*s, dtype=torch.bfloat16) * 1.6)(B, Hkv, Sk, Dh)), 'wq': _w((lambda *s: torch.randn(*s, dtype=torch.bfloat16) * 1.6)(D, Hq * Dh) * D ** (-0.5)), 'wo': _w((lambda *s: torch.randn(*s, dtype=torch.bfloat16) * 1.6)(Hq * Dh, D) * (Hq * Dh) ** (-0.5))}
    _A_in = _A_setup()

    def _B_setup():
        B, S, D, F = (1, 1, 576, 4608)
        return {'x': _w((lambda *s: torch.randn(*s, dtype=torch.bfloat16) * 0.05)(B, S, D)), 'w1': _w((lambda *s: torch.randn(*s, dtype=torch.bfloat16) * 1.6)(S, F) * S ** (-0.5)), 'b1': _w((lambda *s: torch.randn(*s, dtype=torch.bfloat16) * 1.6)(F) * 0.02), 'w2': _w((lambda *s: torch.randn(*s, dtype=torch.bfloat16) * 1.6)(F, S) * F ** (-0.5)), 'b2': _w((lambda *s: torch.randn(*s, dtype=torch.bfloat16) * 1.6)(S) * 0.02)}
    _B_in = _B_setup()
    return {'A_x': _A_in['x'], 'A_k_cache': _A_in['k_cache'], 'A_v_cache': _A_in['v_cache'], 'A_wq': _A_in['wq'], 'A_wo': _A_in['wo'], 'B_x': _B_in['x'], 'B_w1': _B_in['w1'], 'B_b1': _B_in['b1'], 'B_w2': _B_in['w2'], 'B_b2': _B_in['b2'], 'c': _w((lambda *s: torch.randn(*s, dtype=torch.bfloat16) * 0.05)(1, 576) * 8.0), 'w_mod': _w((lambda *s: torch.randn(*s, dtype=torch.bfloat16) * 0.05)(576, 6 * 576) * 576 ** (-0.5) * 8.0)}

def reference(A_x, A_k_cache, A_v_cache, A_wq, A_wo, B_x, B_w1, B_b1, B_w2, B_b2, c, w_mod):

    def _A_ref(x, k_cache, v_cache, wq, wo):
        B, Sq, D = x.shape
        Hq, Dh = (32, 128)
        Hkv, Sk = (k_cache.shape[1], k_cache.shape[2])
        W = min(4096, max(256, Sk // 4))
        rep = Hq // Hkv
        q = (lambda x, *d: x.permute(*d))((x @ wq).reshape(B, Sq, Hq, Dh), 0, 2, 1, 3)
        k = (lambda x, n, axis: torch.repeat_interleave(x, n, dim=axis))(k_cache[:, :, Sk - W:, :], rep, axis=1)
        v = (lambda x, n, axis: torch.repeat_interleave(x, n, dim=axis))(v_cache[:, :, Sk - W:, :], rep, axis=1)
        scores = torch.einsum('bhqd,bhkd->bhqk', q, k) * Dh ** (-0.5)
        p = _softmax(scores, axis=-1)
        o = torch.einsum('bhqk,bhkd->bhqd', p, v)
        return (lambda x, *d: x.permute(*d))(o, 0, 2, 1, 3).reshape(B, Sq, Hq * Dh) @ wo

    def _B_ref(x, w1, b1, w2, b2):
        B, S, D = x.shape
        h = _layernorm(x, axis=-1)
        u = torch.einsum('bsd,sf->bdf', h, w1) + b1
        u = _gelu(u)
        y = torch.einsum('bdf,fs->bds', u, w2) + b2
        return (lambda x, *d: x.permute(*d))(y, 0, 2, 1)
    x = A_x
    mod = (_silu(c) @ w_mod).reshape(c.shape[0], 6, 1, -1)
    sa, ca, ga, sb, cb, gb = (mod[:, 0].clone(), mod[:, 1].clone(), mod[:, 2].clone(), mod[:, 3].clone(), mod[:, 4].clone(), mod[:, 5].clone())
    h = _layernorm(x, axis=-1) * (1 + ca) + sa
    x = x + ga * _A_ref(h, A_k_cache, A_v_cache, A_wq, A_wo)
    h = _layernorm(x, axis=-1) * (1 + cb) + sb
    x = x + gb * _B_ref(h, B_w1, B_b1, B_w2, B_b2)
    return x
```

```cpp
#include <hip/hip_runtime.h>
#include <hip/hip_bf16.h>
#include <stdint.h>
typedef unsigned short u16;
#define MIMIC 0
__device__ __forceinline__ float bf2f(u16 v){ return __uint_as_float(((unsigned)v)<<16); }
__device__ __forceinline__ u16 f2bf(float f){ unsigned u=__float_as_uint(f); return (u16)((u + 0x7FFF + ((u>>16)&1))>>16); }
__device__ __forceinline__ float rb(float f){
 return f;
}
constexpr int D=576, HQ=32, HKV=16, SK=8192, DH=128, W=2048, F=4608;
__global__ void k_mod(const u16* c, const u16* wmod, float* mod){
  int j = blockIdx.x*blockDim.x+threadIdx.x; if(j>=6*D) return;
  float acc=0; for(int i=0;i<D;i++){ float cv=bf2f(c[i]); float s=rb(cv/(1.f+__expf(-cv))); acc+=s*bf2f(wmod[(size_t)i*6*D+j]); }
  mod[j]=rb(acc);
}
__device__ void ln(const float* x, float* y){
  float m=0; for(int i=0;i<D;i++) m+=x[i]; m=rb(m/D);
  float mm=0; for(int i=0;i<D;i++) mm+=x[i]; mm/=D;
  float v=0; for(int i=0;i<D;i++){ float d=x[i]-mm; v+=d*d;} v=rb(v/D);
  float sd=rb(sqrtf(rb(v+1e-5f)));
  for(int i=0;i<D;i++) y[i]=rb(rb(x[i]-m)/sd);
}
__global__ void k_q(const u16* x, const float* mod, const u16* wq, float* q){
  __shared__ float xs[D], h[D];
  if(threadIdx.x==0){ for(int i=0;i<D;i++) xs[i]=bf2f(x[i]); ln(xs,h); for(int i=0;i<D;i++) h[i]=rb(rb(h[i]*rb(1.f+mod[D+i]))+mod[i]); }
  __syncthreads();
  int j = blockIdx.x*blockDim.x+threadIdx.x;
  float acc=0; for(int i=0;i<D;i++) acc+=h[i]*bf2f(wq[(size_t)i*4096+j]);
  q[j]=rb(acc);
}
__global__ void k_attn(const float* q, const u16* K, const u16* V, float* o){
  int hq=blockIdx.x, g=hq/2; __shared__ float s[W]; __shared__ float red[256];
  const u16* k=K+((size_t)g*SK+(SK-W))*DH; const u16* v=V+((size_t)g*SK+(SK-W))*DH;
  float mx=-INFINITY;
  for(int t=threadIdx.x;t<W;t+=256){ float a=0; for(int d=0;d<DH;d++) a+=q[hq*DH+d]*bf2f(k[(size_t)t*DH+d]); a=rb(rb(a)*rb(0.08838834764831845f)); s[t]=a; mx=fmaxf(mx,a);}
  red[threadIdx.x]=mx; __syncthreads();
  for(int i=0;i<256;i++) mx=fmaxf(mx,red[i]); __syncthreads();
  float l=0; for(int t=threadIdx.x;t<W;t+=256){ float e=__expf(s[t]-mx); s[t]=e; l+=e; }
  red[threadIdx.x]=l; __syncthreads(); l=0; for(int i=0;i<256;i++) l+=red[i];
  if(threadIdx.x<128){ float a=0; for(int t=0;t<W;t++) a+=rb(s[t]/l)*bf2f(v[(size_t)t*DH+threadIdx.x]); o[hq*DH+threadIdx.x]=rb(a);}
}
__global__ void k_wo(const float* o, const u16* wo, float* ao){
  int j = blockIdx.x*blockDim.x+threadIdx.x; if(j>=D) return;
  float acc=0; for(int i=0;i<4096;i++) acc+=o[i]*bf2f(wo[(size_t)i*D+j]);
  ao[j]=rb(acc);
}
__global__ void k_mlp(const u16* x, const float* mod, const float* ao, const u16* w1, const u16* b1, const u16* w2, const u16* b2, u16* out){
  __shared__ float x1[D], h[D], h2[D]; __shared__ float red[256];
  if(threadIdx.x==0){ for(int i=0;i<D;i++) x1[i]=rb(bf2f(x[i])+rb(mod[2*D+i]*ao[i])); ln(x1,h); for(int i=0;i<D;i++) h[i]=rb(rb(h[i]*rb(1.f+mod[4*D+i]))+mod[3*D+i]); ln(h,h2);}
  __syncthreads();
  int d=blockIdx.x; float hv=h2[d]; float acc=0;
  for(int f=threadIdx.x;f<F;f+=256){ float u=rb(rb(hv*bf2f(w1[f]))+bf2f(b1[f])); float g=0.5f*u*(1.f+tanhf(0.7978845608028654f*(u+0.044715f*u*u*u))); acc+=rb(g)*bf2f(w2[f]); }
  red[threadIdx.x]=acc; __syncthreads();
  if(threadIdx.x==0){ float a=0; for(int i=0;i<256;i++) a+=red[i]; float y=rb(rb(a)+bf2f(b2[0])); out[d]=f2bf(x1[d]+rb(mod[5*D+d]*y)); }
}
extern "C" void kernel_launch(void* const* d_in, const int* in_sizes, int n_in, void* d_out, int out_size, void* d_ws, size_t ws_size, hipStream_t stream){
  const u16* x=(const u16*)d_in[0]; const u16* K=(const u16*)d_in[1]; const u16* V=(const u16*)d_in[2]; const u16* wq=(const u16*)d_in[3]; const u16* wo=(const u16*)d_in[4];
  const u16* w1=(const u16*)d_in[6]; const u16* b1=(const u16*)d_in[7]; const u16* w2=(const u16*)d_in[8]; const u16* b2=(const u16*)d_in[9]; const u16* c=(const u16*)d_in[10]; const u16* wmod=(const u16*)d_in[11];
  float* ws=(float*)d_ws; float* mod=ws; float* q=ws+4096; float* o=ws+8192; float* ao=ws+12288;
  k_mod<<<54,64,0,stream>>>(c,wmod,mod);
  k_q<<<64,64,0,stream>>>(x,mod,wq,q);
  k_attn<<<32,256,0,stream>>>(q,K,V,o);
  k_wo<<<9,64,0,stream>>>(o,wo,ao);
  k_mlp<<<576,256,0,stream>>>(x,mod,ao,w1,b1,w2,b2,(u16*)d_out);
}
```

```cpp
#include <hip/hip_runtime.h>
#include <stdint.h>

typedef unsigned short u16; typedef unsigned u32; typedef unsigned long long u64;
typedef u32 v4u __attribute__((ext_vector_type(4)));
typedef u32 v2u __attribute__((ext_vector_type(2)));
typedef float v4f __attribute__((ext_vector_type(4)));
typedef __bf16 v8bf __attribute__((ext_vector_type(8)));
typedef __bf16 v2bf __attribute__((ext_vector_type(2)));
typedef short v4s __attribute__((ext_vector_type(4)));
#define LAS __attribute__((address_space(3)))
#define GAS __attribute__((address_space(1)))

#define NT 2
#define WIN 8
#define WO_DELAY 0
constexpr int SC1 = 16;
constexpr u32 SPIN_CAP = 200000;

constexpr u32 WS_H1 = 4096;
constexpr u32 WS_H2 = 40960;
constexpr u32 WS_CL = 131072;
constexpr u32 WS_H3 = WS_CL;
constexpr u32 WS_H4 = WS_H3 + 128 * 1152;
constexpr u32 WS_M2 = WS_H4 + 18432;
constexpr u32 CLN = 152;
constexpr u32 WS_END = WS_CL + 72 * CLN * 16;
static_assert(WS_M2 + 9216 <= WS_END, "");
constexpr u32 WS_ST = 1u << 20;

constexpr u32 L_TR = 0, TRW = 9216;
constexpr u32 L_RED = 8 * TRW;
constexpr u32 L_Q = L_RED + 5120;
constexpr u32 L_ML = L_Q + 528;
constexpr u32 L_ST = L_ML + 128;
constexpr u32 L_X1 = L_ST + 288;
constexpr u32 L_MP = L_X1 + 2304;
constexpr u32 L_RED2 = L_MP + 128;
constexpr u32 L_END = L_RED2 + 2048;
constexpr u32 LDS_BYTES = 84 * 1024;
static_assert(L_END <= LDS_BYTES, "");

__device__ __forceinline__ float asf(u32 x) { return __builtin_bit_cast(float, x); }
__device__ __forceinline__ u32 asu(float x) { return __builtin_bit_cast(u32, x); }
__device__ __forceinline__ float bf2f(u16 v) { return asf((u32)v << 16); }
__device__ __forceinline__ u32 pk(float lo, float hi) { v2bf r = {(__bf16)lo, (__bf16)hi}; return __builtin_bit_cast(u32, r); }
__device__ __forceinline__ float rb(float x) { return asf(pk(x, x) << 16); }
__device__ __forceinline__ float lo16(u32 w) { return asf(w << 16); }
__device__ __forceinline__ float hi16(u32 w) { return asf(w & 0xFFFF0000u); }
template <int C> __device__ __forceinline__ float dpp(float x) { return asf((u32)__builtin_amdgcn_mov_dpp((int)asu(x), C, 0xf, 0xf, true)); }
template <int C> __device__ __forceinline__ u32 dppu(u32 x) { return (u32)__builtin_amdgcn_mov_dpp((int)x, C, 0xf, 0xf, true); }
constexpr int XOR1 = 0xB1, XOR2 = 0x4E, HMIR = 0x141, ROR4 = 0x124, ROR8 = 0x128, SHL4 = 0x104, QB0 = 0x00, QB1 = 0x55, QB2 = 0xAA, QB3 = 0xFF;
__device__ __forceinline__ float sw32(float a, float b) { auto r = __builtin_amdgcn_permlane32_swap(asu(a), asu(b), false, false); return asf(r[0]) + asf(r[1]); }
__device__ __forceinline__ float sw16(float a, float b) { auto r = __builtin_amdgcn_permlane16_swap(asu(a), asu(b), false, false); return asf(r[0]) + asf(r[1]); }
__device__ __forceinline__ float mx32(float a) { auto r = __builtin_amdgcn_permlane32_swap(asu(a), asu(a), false, false); return fmaxf(asf(r[0]), asf(r[1])); }
__device__ __forceinline__ float mx16(float a) { auto r = __builtin_amdgcn_permlane16_swap(asu(a), asu(a), false, false); return fmaxf(asf(r[0]), asf(r[1])); }
__device__ __forceinline__ float row_sum(float x) { x += dpp<XOR1>(x); x += dpp<XOR2>(x); x += dpp<HMIR>(x); x += dpp<ROR8>(x); return x; }
__device__ __forceinline__ float wave_sum(float x) { x = row_sum(x); x = sw16(x, x); return sw32(x, x); }
__device__ __forceinline__ float rl(float x, int k) { return asf((u32)__builtin_amdgcn_readlane((int)asu(x), k)); }
__device__ __forceinline__ v4f mfma(v4u a, v4u b, v4f c) { return __builtin_amdgcn_mfma_f32_16x16x32_bf16(__builtin_bit_cast(v8bf, a), __builtin_bit_cast(v8bf, b), c, 0, 0, 0); }
__device__ __forceinline__ void bar() { asm volatile("s_waitcnt lgkmcnt(0)\n\ts_barrier" ::: "memory"); }
__device__ __forceinline__ u32 clean(float x) { return min(asu(x), 0xFFFFFFFEu); }
#define FENCE() asm volatile("" ::: "memory")
#define SCHED() __builtin_amdgcn_sched_barrier(0)
typedef __amdgpu_buffer_rsrc_t rsrc_t;
__device__ __forceinline__ rsrc_t mkr(const void* p, u32 bytes) { return __builtin_amdgcn_make_buffer_rsrc((void*)p, 0, bytes, 0x00020000); }
template <int AUX> __device__ __forceinline__ v4u ld16(rsrc_t r, u32 vo, u32 so) { return __builtin_amdgcn_raw_buffer_load_b128(r, vo, so, AUX); }
template <int AUX> __device__ __forceinline__ v2u ld8(rsrc_t r, u32 vo, u32 so) { return __builtin_amdgcn_raw_buffer_load_b64(r, vo, so, AUX); }
template <int AUX> __device__ __forceinline__ u32 ld4(rsrc_t r, u32 vo, u32 so) { return __builtin_amdgcn_raw_buffer_load_b32(r, vo, so, AUX); }
__device__ __forceinline__ float ldbf(rsrc_t r, u32 idx) { return bf2f(__builtin_amdgcn_raw_buffer_load_b16(r, idx * 2, 0, 0)); }

#define STAMP(k) do {} while (0)
#define FLUSH() do {} while (0)

__device__ __forceinline__ float silu_bf(float cv) { return rb(cv * __builtin_amdgcn_rcpf(1.f + __builtin_amdgcn_exp2f(-1.4426950408889634f * cv))); }

__device__ __forceinline__ v4u bdelta(float mult, const u32 (&msk)[4]) { u32 p = pk(mult, mult); return v4u{p & msk[0], p & msk[1], p & msk[2], p & msk[3]}; }

__device__ __forceinline__ void red_write(LAS char* lds, v4f acc, int w, int l) {
  if ((l >> 5) == ((l >> 3) & 1)) *(LAS v4f*)(lds + L_RED + ((w * 2 + (l >> 5)) * 64 + ((l >> 4) & 1) * 32 + (l & 7) * 4) * 4) = acc;
}
__device__ __forceinline__ float red_read(LAS char* lds, int l) {
  LAS v4f* p = (LAS v4f*)(lds + L_RED + ((l >> 4) * 64 + (l & 15) * 4) * 4);
  v4f a = p[0], b = p[64], c = p[128], d = p[192], e = p[256];
  a = (a + b) + (c + d) + e;
  return sw16(sw32(a[0], a[1]), sw32(a[2], a[3]));
}
__device__ __forceinline__ v4f red_read4(LAS char* lds, int l) {
  LAS v4f* p = (LAS v4f*)(lds + L_RED + ((l >> 4) * 64 + (l & 15) * 4) * 4);
  v4f a = p[0], b = p[64], c = p[128], d = p[192], e = p[256];
  a = (a + b) + (c + d) + e;
#pragma unroll
  for (int k = 0; k < 4; ++k) { a[k] = sw16(a[k], a[k]); a[k] = sw32(a[k], a[k]); }
  return a;
}
__device__ __forceinline__ int col_lane(int c) { return (c & 7) | ((c >> 5) & 1) << 3 | ((c >> 4) & 1) << 4 | ((c >> 3) & 1) << 5; }
__device__ __forceinline__ int col_idx(int c) { return (c & ~31) + 4 * (c & 7) + ((c >> 3) & 3); }
__device__ __forceinline__ int red_col(int l) { int idx = 4 * (l & 15) + (l >> 5) + 2 * ((l >> 4) & 1); return (idx & 32) + (idx & 3) * 8 + ((idx >> 2) & 7); }

__global__ void __launch_bounds__(576, 3)
adaln_block_kernel(const u16* __restrict__ gx, const char* __restrict__ gK, const char* __restrict__ gV, const char* __restrict__ gwq,
                   const char* __restrict__ gwo, const u16* __restrict__ gw1, const u16* __restrict__ gb1, const u16* __restrict__ gw2,
                   const u16* __restrict__ gb2, const u16* __restrict__ gc, const char* __restrict__ gwm, u16* __restrict__ gout, char* gws) {
  __shared__ __attribute__((aligned(16))) char smem[LDS_BYTES];
  LAS char* lds = (LAS char*)smem;
  const int b = blockIdx.x, x = b & 7, ip = b >> 4, t = threadIdx.x;
  const int w = __builtin_amdgcn_readfirstlane(t >> 6), l = t & 63;
  const bool attn = ((b >> 3) & 1) == 0;
  const rsrc_t rws = mkr(gws, 4u << 20);
  const rsrc_t rx = mkr(gx, 1152), rc = mkr(gc, 1152);
  asm volatile("" :: "s"(gx), "s"(gK), "s"(gV), "s"(gwq), "s"(gwo), "s"(gw1), "s"(gb1), "s"(gw2), "s"(gb2), "s"(gc), "s"(gwm), "s"(gout), "s"(gws));
  STAMP(0);
  const v2u ep = ld8<SC1>(rws, 0, 0);
  int mi;
  u32 T0, T1;

  if (attn) {
    const int g = 2 * x + (ip >> 3), s = ip & 7;
    mi = 16 * x + ip;
    if (w < 8) {
      const u32 rowb = (u32)(g * 8192 + 6144 + s * 256 + w * 32) * 256u;
      const rsrc_t rK = mkr(gK, 32u << 20), rV = mkr(gV, 32u << 20);
      const u32 vo = l * 16;
      const u32 ko = ((l >> 2) & 3) * 256 + ((l >> 4) * 4 + (l & 3)) * 16;
      v4u D[16];
      LAS char* tr = lds + L_TR + w * TRW;
#pragma unroll
      for (int i = 0; i < 16 + WIN; ++i) {
        if (i < 16) D[i] = i < 8 ? ld16<NT>(rV, vo, rowb + i * 1024) : ld16<NT>(rK, ko, rowb + (i - 8) * 1024);
        SCHED();
        const int j = i - WIN + 1;
        if (i < 15 && j >= 0) {
          if (j < 8) *(LAS v4u*)(tr + (4 * j + (l >> 4)) * 272 + (l & 15) * 16) = D[j];
          else asm volatile("s_waitcnt vmcnt(%0)" :: "i"(WIN - 1) : "memory");
        } else if (j >= 0 && j < 8) *(LAS v4u*)(tr + (4 * j + (l >> 4)) * 272 + (l & 15) * 16) = D[j];
        SCHED();
      }
      STAMP(1);
      v4u VB[8];
      { LAS char* a0 = tr + (4 * ((l >> 2) & 3) + (l >> 4)) * 272 + (l & 3) * 8;
#pragma unroll
        for (int n = 0; n < 8; ++n) {
          v2u lo = __builtin_bit_cast(v2u, __builtin_amdgcn_ds_read_tr16_b64_v4i16((LAS v4s*)(a0 + n * 32)));
          v2u hi = __builtin_bit_cast(v2u, __builtin_amdgcn_ds_read_tr16_b64_v4i16((LAS v4s*)(a0 + n * 32 + 16 * 272)));
          VB[n] = v4u{lo[0], lo[1], hi[0], hi[1]};
        } }
      const u32 qa = L_Q + ((l >> 3) & 1) * 256 + ((l >> 4) * 4 + (l & 3)) * 16;
      const u32 qa0 = (l & 4) ? L_Q + 512 : qa, qa1 = (l & 4) ? qa : L_Q + 512;
      STAMP(2);
      bar();
      STAMP(3);
      const v4u B0 = *(LAS v4u*)(lds + qa0), B1 = *(LAS v4u*)(lds + qa1);
      float sc[4];
#pragma unroll
      for (int q = 0; q < 4; ++q) {
        v4f d = mfma(D[8 + q], B0, v4f{0.f, 0.f, 0.f, 0.f});
        d = mfma(D[12 + q], B1, d);
        sc[q] = (dpp<QB0>(d[0]) + dpp<QB1>(d[1])) + (dpp<QB2>(d[2]) + dpp<QB3>(d[3]));
      }
      { u32 a = pk(sc[0], sc[1]), c = pk(sc[2], sc[3]);
        const float SC = 0.08838834764831845f;
        a = pk(lo16(a) * SC, hi16(a) * SC); c = pk(lo16(c) * SC, hi16(c) * SC);
        sc[0] = lo16(a); sc[1] = hi16(a); sc[2] = lo16(c); sc[3] = hi16(c); }
      float m = fmaxf(fmaxf(sc[0], sc[1]), fmaxf(sc[2], sc[3]));
      m = fmaxf(m, dpp<HMIR>(m)); m = mx16(m); m = mx32(m);
      const float L2E = 1.4426950408889634f, m2 = m * L2E;
      float p[4];
#pragma unroll
      for (int q = 0; q < 4; ++q) p[q] = __builtin_amdgcn_exp2f(fmaf(sc[q], L2E, -m2));
      const u32 wa = pk(p[0], p[1]), wb = pk(p[2], p[3]);
      const v4u PA = {wa, wb, dppu<SHL4>(wa), dppu<SHL4>(wb)};
      float ls = (p[0] + p[1]) + (p[2] + p[3]);
      ls += dpp<HMIR>(ls); ls = sw16(ls, ls); ls = sw32(ls, ls);
      LAS float* ow = (LAS float*)(tr + (l >> 5) * 512) + (l & 15);
#pragma unroll
      for (int n = 0; n < 8; ++n) {
        v4f o = mfma(PA, VB[n], v4f{0.f, 0.f, 0.f, 0.f});
        if (!(l & 16)) ow[n * 16] = o[0];
      }
      if ((l & 55) == 0) { LAS float* ml = (LAS float*)(lds + L_ML) + (l >> 3) * 16 + w; ml[0] = m2; ml[8] = ls; }
      STAMP(4);
      bar();
      if (w < 5) {
        LAS v4f* ml = (LAS v4f*)(lds + L_ML + (w < 4 ? (w >> 1) : (l >> 5)) * 64);
        const v4f ma = ml[0], mb = ml[1], la = ml[2], lb = ml[3];
        float ov[8];
#pragma unroll
        for (int k = 0; k < 8; ++k) ov[k] = *(LAS float*)(lds + L_TR + k * TRW + (t & 255) * 4);
        const float M = fmaxf(fmaxf(fmaxf(ma[0], ma[1]), fmaxf(ma[2], ma[3])), fmaxf(fmaxf(mb[0], mb[1]), fmaxf(mb[2], mb[3])));
        float o = 0.f, L = 0.f;
#pragma unroll
        for (int k = 0; k < 8; ++k) { const float f = __builtin_amdgcn_exp2f((k < 4 ? ma[k & 3] : mb[k & 3]) - M); o = fmaf(f, ov[k], o); L = fmaf(f, k < 4 ? la[k & 3] : lb[k & 3], L); }
        if (w < 4) {
          const u32 oc = clean(o);
          const v4u o4 = {oc, dppu<QB1>(oc), dppu<QB2>(oc), dppu<QB3>(oc)};
          if (!(l & 3)) __builtin_amdgcn_raw_buffer_store_b128(o4, rws, t * 4, WS_H3 + (g * 8 + s) * 1152, SC1);
        } else {
          const auto r = __builtin_amdgcn_permlane32_swap(clean(M), clean(L), false, false);

          if (l == 0) __builtin_amdgcn_raw_buffer_store_b128(v4u{clean(M), clean(L), r[1], (u32)__builtin_amdgcn_readlane((int)r[1], 32)}, rws, 1024, WS_H3 + (g * 8 + s) * 1152, SC1);
        }
      }
      STAMP(5);
    } else {
      __builtin_amdgcn_s_setprio(3);
      { const u64 e = (((u64)ep[1] << 32) | ep[0]) + 1; T0 = __builtin_amdgcn_readfirstlane((u32)e); T1 = __builtin_amdgcn_readfirstlane((u32)(e >> 32)) & 0xFFu; }
      if (l < 4) *(LAS u32*)(lds + L_Q + 512 + l * 4) = 0;
      v4u G[4];
      _Pragma("nounroll") for (u32 spin = 0; spin < SPIN_CAP; ++spin) {
        FENCE();
#pragma unroll
        for (int k = 0; k < 4; ++k) G[k] = ld16<SC1>(rws, l * 16, WS_H2 + g * 4096 + k * 1024);
        u32 bad = 0;
#pragma unroll
        for (int k = 0; k < 4; ++k) bad |= (G[k][0] ^ T0) | (G[k][2] ^ T0) | (((G[k][1] ^ T1) | (G[k][3] ^ T1)) & 0xFFu);
        if (__all(bad == 0)) break;
      }
      STAMP(2);
      const int col = red_col(2 * (l & 31) + (l >> 5));
#pragma unroll
      for (int k = 0; k < 4; ++k) {
        const float q = sw32(asf(G[k][1] & 0xFFFFFF00u), asf(G[k][3] & 0xFFFFFF00u));
        *(LAS u16*)(lds + L_Q + (k * 64 + col) * 2) = (u16)pk(q, q);
      }
      __builtin_amdgcn_s_setprio(0);
      bar();
      STAMP(3);
      bar();
    }
  } else {
    const bool s1 = ip <= 8;
    mi = ip == 8 ? 184 + x : (ip > 8 ? 128 + (ip - 9) * 8 + x : -1);
    const rsrc_t rwm = mkr(gwm, 576 * 6912), rwq = mkr(gwq, 576 * 8192);
    const u32 rsub = l >> 3, chunk = (l & 7) * 16;
    const int j1 = ip * 8 + x, lc = j1 >> 2, rq = j1 & 3;
    const int ct = 8 * x + (ip >> 1), hf = ip & 1;
    v4u A1[2], A2[4];
    u32 c1[2];
    { const rsrc_t rcl = mkr(gws, s1 ? WS_END : 0), rw1 = mkr(gwm, s1 ? 576 * 6912 : 0);
      __builtin_amdgcn_raw_buffer_store_b128(v4u{~0u, ~0u, ~0u, ~0u}, rcl, t < CLN ? WS_CL + (j1 * CLN + t) * 16 : WS_END, 0, SC1);
      FENCE(); SCHED();
#pragma unroll
      for (int i = 0; i < 2; ++i) A1[i] = ld16<NT>(rw1, (w * 16 + i * 8 + rsub) * 6912 + chunk, rq * 144 * 6912 + lc * 128);
      SCHED();
#pragma unroll
      for (int i = 0; i < 2; ++i) c1[i] = __builtin_amdgcn_raw_buffer_load_b16(rc, (rq * 144 + w * 16 + i * 8 + rsub) * 2, 0, 0);
    }
    SCHED();
#pragma unroll
    for (int i = 0; i < 4; ++i) A2[i] = ld16<NT>(rwq, (w * 32 + i * 8 + rsub) * 8192 + chunk, hf * 288 * 8192 + ct * 128);
    const v4u xa = ld16<0>(rx, l * 16, 0), xb = ld16<0>(rx, l * 16, 1024);
    float xr[4];
#pragma unroll
    for (int i = 0; i < 4; ++i) xr[i] = ldbf(rx, hf * 288 + w * 32 + i * 8 + rsub);
    SCHED();
    STAMP(1);
    u32 msk[4];
#pragma unroll
    for (int r = 0; r < 4; ++r) msk[r] = ((l & 7) >> 1) == r ? ((l & 1) ? 0xFFFF0000u : 0xFFFFu) : 0u;
    { const u64 e = (((u64)ep[1] << 32) | ep[0]) + 1; T0 = __builtin_amdgcn_readfirstlane((u32)e); T1 = __builtin_amdgcn_readfirstlane((u32)(e >> 32)) & 0xFFu; }
    if (t < 128) *(LAS float*)(lds + L_RED + 18 * 256 + t * 4) = 0.f;
    if (s1) {
      v4f acc = {0.f, 0.f, 0.f, 0.f};
#pragma unroll
      for (int i = 0; i < 2; ++i) acc = mfma(A1[i], bdelta(silu_bf(lo16(c1[i])), msk), acc);
      red_write(lds, acc, w, l);
      STAMP(2);
      bar();
      if (w == 0) {
        const float v = red_read(lds, l);
        const u32 hi = ((asu(v) + 0x80u) & 0xFFFFFF00u) | T1;
        __builtin_amdgcn_raw_buffer_store_b64(v2u{T0, hi}, rws, WS_H1 + (rq * 1152 + lc * 64 + l) * 8, 0, SC1);
      }
      STAMP(3);
    }
    { float sm = 0.f, sv = 0.f;
#pragma unroll
      for (int i = 0; i < 8; ++i) { const u32 u = i < 4 ? xa[i & 3] : xb[i & 3]; const float a = lo16(u), c = hi16(u); sm += a + c; sv = fmaf(a, a, fmaf(c, c, sv)); }
      const float tot = row_sum(sw16(sw32(sm, sv), sw32(sm, sv)));
      const float mu = rl(tot, 0) * (1.f / 576.f);
      const float var = fmaxf(fmaf(-mu, mu, rl(tot, 32) * (1.f / 576.f)), 0.f);
      const float mb = rb(mu), isd = 1.f / rb(sqrtf(rb(rb(var) + 1e-5f)));
#pragma unroll
      for (int i = 0; i < 4; ++i) xr[i] = rb(rb(xr[i] - mb) * isd); }
    if (s1) bar();
    v2u G[4];
    { u32 go[4];
#pragma unroll
      for (int i = 0; i < 4; ++i) { const int cc = hf * 288 + w * 32 + i * 8 + rsub; go[i] = WS_H1 + ((l & 3) * 1152 + ((l >> 2) & 1) * 576 + (cc & ~63) + col_lane(cc & 63)) * 8; }
      _Pragma("nounroll") for (u32 spin = 0; spin < SPIN_CAP; ++spin) {
        FENCE();
#pragma unroll
        for (int i = 0; i < 4; ++i) G[i] = ld8<SC1>(rws, go[i], 0);
        u32 bad = 0;
        if (w == 8) {
          const v2u e0 = ld8<SC1>(rws, WS_H1 + ((l & 3) * 1152 + (l >> 2) * 64) * 8, 0);
          const v2u e1 = ld8<SC1>(rws, WS_H1 + ((l & 3) * 1152 + (16 + ((l >> 2) & 1)) * 64) * 8, 0);
          bad = (e0[0] ^ T0) | (e1[0] ^ T0) | (((e0[1] ^ T1) | (e1[1] ^ T1)) & 0xFFu);
        }
#pragma unroll
        for (int i = 0; i < 4; ++i) bad |= (G[i][0] ^ T0) | ((G[i][1] ^ T1) & 0xFFu);
        if (__all(bad == 0)) break;
      } }
    STAMP(4);
    { v4f acc = {0.f, 0.f, 0.f, 0.f};
#pragma unroll
      for (int i = 0; i < 4; ++i) {
        float v = asf(G[i][1] & 0xFFFFFF00u);
        v += dpp<XOR1>(v); v += dpp<XOR2>(v); v = rb(v);
        const float o = dpp<HMIR>(v);
        const float sa = (l & 4) ? o : v, ca = (l & 4) ? v : o;
        const float h = rb(xr[i] * rb(1.f + ca)) + sa;
        acc = mfma(A2[i], bdelta(h, msk), acc);
      }
      red_write(lds, acc, w, l); }
    bar();
    if (w == 0) {
      const float v = red_read(lds, l);
      const u32 hi = ((asu(v) + 0x80u) & 0xFFFFFF00u) | T1;
      __builtin_amdgcn_raw_buffer_store_b64(v2u{T0, hi}, rws, WS_H2 + ((ct * 2 + hf) * 64 + l) * 8, 0, SC1);
    }
    STAMP(5);
    if (WO_DELAY) __builtin_amdgcn_s_sleep(WO_DELAY);
    if (s1) {
      if (w < 8) {
        const rsrc_t rwo = mkr(gwo, 4096 * 1152);
        LAS char* tr = lds + L_TR + w * TRW;
        v4u D[8];
#pragma unroll
        for (int i = 0; i < 8; ++i) D[i] = ld16<NT>(rwo, (i * 8 + rsub) * 1152 + chunk, (x * 512 + w * 64) * 1152 + ip * 128);
#pragma unroll
        for (int i = 0; i < 8; ++i) *(LAS v4u*)(tr + (i * 8 + rsub) * 144 + chunk) = D[i];
        v4u WB[8];
        { LAS char* a0 = tr + (32 * (l >> 5) + ((l >> 4) & 1) + 4 * ((l >> 3) & 1) + 2 * ((l >> 2) & 1)) * 144 + (l & 3) * 8;
#pragma unroll
          for (int n = 0; n < 8; ++n) {
            LAS char* a = a0 + (n >> 2) * 16 * 144 + (n & 3) * 32;
            v2u lo = __builtin_bit_cast(v2u, __builtin_amdgcn_ds_read_tr16_b64_v4i16((LAS v4s*)a));
            v2u hi = __builtin_bit_cast(v2u, __builtin_amdgcn_ds_read_tr16_b64_v4i16((LAS v4s*)(a + 8 * 144)));
            WB[n] = v4u{lo[0], lo[1], hi[0], hi[1]};
          } }
        STAMP(6);
        v4u O0, O1; v2u ML;
        const u32 so = WS_H3 + (2 * x + (w >> 2)) * 8 * 1152;
        const u32 oo = (l >> 3) * 1152 + (w & 3) * 256 + (l & 7) * 16, mo = (l >> 3) * 1152 + 1024 + ((w >> 1) & 1) * 8;
        _Pragma("nounroll") for (u32 spin = 0; spin < SPIN_CAP; ++spin) {
          FENCE();
          ML = ld8<SC1>(rws, mo, so);
          O0 = ld16<SC1>(rws, oo, so); O1 = ld16<SC1>(rws, oo + 128, so);
          const u32 mx = max(max(max(max(O0[0], O0[1]), O0[2]), max(max(O0[3], O1[0]), O1[1])), max(max(max(O1[2], O1[3]), ML[0]), ML[1]));
          if (__all(mx != 0xFFFFFFFFu)) break;
        }
        STAMP(7);
        float M = asf(ML[0]);
        M = fmaxf(M, dpp<ROR8>(M)); M = mx16(M); M = mx32(M);
        const float f = __builtin_amdgcn_exp2f(asf(ML[0]) - M);
        float L = f * asf(ML[1]);
        L += dpp<ROR8>(L); L = sw16(L, L); L = sw32(L, L);
        const float iL = __builtin_amdgcn_rcpf(L);
        float oa = sw16(sw32(f * asf(O0[0]), f * asf(O1[0])), sw32(f * asf(O0[1]), f * asf(O1[1])));
        float ob = sw16(sw32(f * asf(O0[2]), f * asf(O1[2])), sw32(f * asf(O0[3]), f * asf(O1[3])));
        oa += dpp<ROR8>(oa); ob += dpp<ROR8>(ob);
        const u32 W = pk(oa * iL, ob * iL);
        v4u OA[2];
        OA[0] = v4u{W, dppu<0x101>(W), dppu<0x102>(W), dppu<0x103>(W)};
        OA[1] = v4u{dppu<0x104>(W), dppu<0x105>(W), dppu<0x106>(W), dppu<0x107>(W)};
#pragma unroll
        for (int nt = 0; nt < 4; ++nt) {
          v4f d = mfma(OA[0], WB[nt], v4f{0.f, 0.f, 0.f, 0.f});
          d = mfma(OA[1], WB[4 + nt], d);
          if (l < 16) *(LAS float*)(lds + L_RED2 + (w * 64 + nt * 16 + l) * 4) = d[0];
        }
      }
      STAMP(8);
      bar();
      if (w == 0) {
        LAS v4f* p = (LAS v4f*)(lds + L_RED2 + ((l >> 4) * 64 + (l & 15) * 4) * 4);
        v4f a = p[0] + p[64];
#pragma unroll
        for (int k = 0; k < 4; ++k) { a[k] = sw16(a[k], a[k]); a[k] = sw32(a[k], a[k]); }
        if (l < 16) __builtin_amdgcn_raw_buffer_store_b128(v4u{clean(a[0]), clean(a[1]), clean(a[2]), clean(a[3])}, rws, WS_H4 + (x * 576 + ip * 64 + l * 4) * 4, 0, SC1);
      }
      STAMP(9);
    } else {
      const int j2 = (ip - 9) * 8 + x;
      if (j2 < 36) {
        float cm[8]; v4u D[8];
#pragma unroll
        for (int i = 0; i < 8; ++i) cm[i] = ldbf(rc, w * 64 + i * 8 + rsub);
#pragma unroll
        for (int i = 0; i < 8; ++i) D[i] = ld16<NT>(rwm, (w * 64 + i * 8 + rsub) * 6912 + chunk, 2304 + j2 * 128);
        bar();
        v4f acc = {0.f, 0.f, 0.f, 0.f};
#pragma unroll
        for (int i = 0; i < 8; ++i) acc = mfma(D[i], bdelta(silu_bf(cm[i]), msk), acc);
        red_write(lds, acc, w, l);
        bar();
        if (w == 0) { const v4f a = red_read4(lds, l);
          if (l < 16) __builtin_amdgcn_raw_buffer_store_b128(v4u{clean(rb(a[0])), clean(rb(a[1])), clean(rb(a[2])), clean(rb(a[3]))}, rws, WS_M2 + (j2 * 64 + l * 4) * 4, 0, SC1); }
        STAMP(9);
      }
    }
  }
  if (mi < 0) { FLUSH(); return; }

  float cA3[9], cA2[9], cA1[9], cA0[9], cP[9], cQ[9];
  if (w < 8) {
    const rsrc_t r1 = mkr(gw1, 9216), r2 = mkr(gb1, 9216), r3 = mkr(gw2, 9216);
    const v4u a = ld16<0>(r1, t * 16, 0), c = ld16<0>(r2, t * 16, 0), d = ld16<0>(r3, t * 16, 0);
    const float ae = ldbf(r1, 4096 + t), ce = ldbf(r2, 4096 + t), de = ldbf(r3, 4096 + t);
    const float K1 = -2.302208198144325f, K2 = K1 * 0.044715f;
#pragma unroll
    for (int f = 0; f < 9; ++f) {
      const float wv = f == 8 ? ae : ((f & 1) ? hi16(a[f >> 1]) : lo16(a[f >> 1]));
      const float bv = f == 8 ? ce : ((f & 1) ? hi16(c[f >> 1]) : lo16(c[f >> 1]));
      const float uv = f == 8 ? de : ((f & 1) ? hi16(d[f >> 1]) : lo16(d[f >> 1]));
      const float kw = K2 * wv, kww = kw * wv, bb = bv * bv;
      cA3[f] = kww * wv; cA2[f] = 3.f * kww * bv; cA1[f] = fmaf(3.f * kw, bb, K1 * wv); cA0[f] = bv * fmaf(K2, bb, K1);
      cP[f] = wv * uv; cQ[f] = bv * uv;
    }
  }
  const int col = 64 * w + 4 * (l & 15) + (l >> 5) + 2 * ((l >> 4) & 1), row = 3 * mi + min(l & 3, 2);
  const float xc = ldbf(rx, col), b2 = ldbf(mkr(gb2, 2), 0);
  float ga, ca, cb, ar, br, gr;
  _Pragma("nounroll") for (u32 spin = 0; spin < SPIN_CAP; ++spin) {
    FENCE();
    const u32 co = WS_M2 + col_idx(col) * 4, ro = WS_M2 + col_idx(row) * 4;
    const u32 g0 = ld4<SC1>(rws, co, 0), g1 = ld4<SC1>(rws, co, 2304), g2 = ld4<SC1>(rws, co, 4608);
    const u32 g3 = ld4<SC1>(rws, ro, 2304), g4 = ld4<SC1>(rws, ro, 4608), g5 = ld4<SC1>(rws, ro, 6912);
    ga = asf(g0); cb = asf(g1); ca = asf(g2); br = asf(g3); ar = asf(g4); gr = asf(g5);
    if (__all(max(max(max(g0, g1), g2), max(max(g3, g4), g5)) != 0xFFFFFFFFu)) break;
    __builtin_amdgcn_s_sleep(8);
  }
  ca = rb(1.f + ca); ar = rb(1.f + ar);
  STAMP(10);
  LAS float* st = (LAS float*)(lds + L_ST);
  { const float A = sw16(sw32(ca, ca * ca), sw32(ca * cb, cb)), B = sw32(cb * cb, cb * cb);
    const float As = row_sum(A), Bs = row_sum(sw16(B, B));
    if (!(l & 15)) { st[w * 8 + (l >> 5) + 2 * ((l >> 4) & 1)] = As; st[w * 8 + 4] = Bs; } }
  bar();
  float Ca, Caa, Cab, Cb, Cbb;
  { float v = st[l]; v += dpp<ROR8>(v); v = sw16(v, v); v = sw32(v, v); v += st[64 + (l & 7)];
    Ca = rl(v, 0); Caa = rl(v, 1); Cab = rl(v, 2); Cb = rl(v, 3); Cbb = rl(v, 4); }
  bar();
  STAMP(11);
  v4u P0, P1;
  { const u32 po = WS_H4 + ((l >> 4) * 576 + w * 64 + (l & 15) * 4) * 4;
    _Pragma("nounroll") for (u32 spin = 0; spin < SPIN_CAP; ++spin) {
      FENCE();
      P0 = ld16<SC1>(rws, po, 0); P1 = ld16<SC1>(rws, po, 9216);
      const u32 mx = max(max(max(max(P0[0], P0[1]), P0[2]), max(max(P0[3], P1[0]), P1[1])), max(P1[2], P1[3]));
      if (__all(mx != 0xFFFFFFFFu)) break;
    } }
  STAMP(12);
  { const float ao = sw16(sw32(asf(P0[0]) + asf(P1[0]), asf(P0[1]) + asf(P1[1])), sw32(asf(P0[2]) + asf(P1[2]), asf(P0[3]) + asf(P1[3])));
    const float x1 = rb(xc + rb(ga * rb(ao)));
    const float xa = x1 * ca;
    const float A = sw16(sw32(x1, x1 * x1), sw32(xa, xa * xa)), B0 = sw32(xa * ca, xa * cb);
    const float As = row_sum(A), Bs = row_sum(sw16(B0, B0));
    *(LAS float*)(lds + L_X1 + col * 4) = x1;
    if (!(l & 15)) { st[w * 8 + (l >> 5) + 2 * ((l >> 4) & 1)] = As; st[w * 8 + 4 + (l >> 5)] = Bs; } }
  bar();
  STAMP(13);
  if (w == 8) { FLUSH(); return; }
  const float x1r = *(LAS float*)(lds + L_X1 + row * 4);
  float h0, h1, h2;
  { float v = st[l]; v += dpp<ROR8>(v); v = sw16(v, v); v = sw32(v, v); v += st[64 + (l & 7)];
    const float S1 = rl(v, 0), S2 = rl(v, 1), S3 = rl(v, 2), S4 = rl(v, 3), S5 = rl(v, 4), S6 = rl(v, 5);
    const float in = 1.f / 576.f, mu = S1 * in;
    const float r = __builtin_amdgcn_rsqf(fmaxf(fmaf(-mu, mu, S2 * in), 0.f) + 1e-5f);
    const float muh = fmaf(fmaf(-mu, Ca, S3), r, Cb) * in;
    const float e2 = fmaf(fmaf(mu, fmaf(mu, Caa, -2.f * S5), S4), r * r, fmaf(2.f * r, fmaf(-mu, Cab, S6), Cbb)) * in;
    const float rh = __builtin_amdgcn_rsqf(fmaxf(fmaf(-muh, muh, e2), 0.f) + 1e-5f);
    const float hv = (fmaf((x1r - mu) * r, ar, br) - muh) * rh;
    h0 = rl(hv, 0); h1 = rl(hv, 1); h2 = rl(hv, 2); }
  STAMP(14);
  float y0 = 0.f, y1 = 0.f, y2 = 0.f;
#pragma unroll
  for (int f = 0; f < 9; ++f) {
    y0 = fmaf(fmaf(h0, cP[f], cQ[f]), __builtin_amdgcn_rcpf(1.f + __builtin_amdgcn_exp2f(fmaf(fmaf(fmaf(cA3[f], h0, cA2[f]), h0, cA1[f]), h0, cA0[f]))), y0);
    y1 = fmaf(fmaf(h1, cP[f], cQ[f]), __builtin_amdgcn_rcpf(1.f + __builtin_amdgcn_exp2f(fmaf(fmaf(fmaf(cA3[f], h1, cA2[f]), h1, cA1[f]), h1, cA0[f]))), y1);
    y2 = fmaf(fmaf(h2, cP[f], cQ[f]), __builtin_amdgcn_rcpf(1.f + __builtin_amdgcn_exp2f(fmaf(fmaf(fmaf(cA3[f], h2, cA2[f]), h2, cA1[f]), h2, cA0[f]))), y2);
  }
  { const float C = row_sum(sw16(sw32(y0, y1), sw32(y2, y2)));
    if (!(l & 15)) *(LAS float*)(lds + L_MP + (w * 4 + (l >> 5) + 2 * ((l >> 4) & 1)) * 4) = C; }
  STAMP(15);
  bar();
  if (w == 0) {
    float v = *(LAS float*)(lds + L_MP + (l & 31) * 4);
    v += dpp<ROR4>(v); v += dpp<ROR8>(v); v = sw16(v, v);
    const float y = rb(rb(v) + b2);
    const float o = x1r + rb(gr * y);
    if (l < 3) gout[row] = (u16)pk(o, o);
    if (b == 0 && l == 0) { const u64 e = (((u64)ep[1] << 32) | ep[0]) + 1; __builtin_amdgcn_raw_buffer_store_b64(v2u{(u32)e, (u32)(e >> 32)}, rws, 0, 0, SC1); }
    STAMP(16);
  }
  FLUSH();
}

extern "C" void kernel_launch(void* const* d_in, const int* in_sizes, int n_in, void* d_out, int out_size, void* d_ws, size_t ws_size, hipStream_t stream) {
  adaln_block_kernel<<<256, 576, 0, stream>>>((const u16*)d_in[0], (const char*)d_in[1], (const char*)d_in[2], (const char*)d_in[3], (const char*)d_in[4],
                                              (const u16*)d_in[6], (const u16*)d_in[7], (const u16*)d_in[8], (const u16*)d_in[9], (const u16*)d_in[10],
                                              (const char*)d_in[11], (u16*)d_out, (char*)d_ws);
}
```

```cpp
#include <hip/hip_runtime.h>
typedef unsigned u32;
#define D(n) const void* p##n, u32 s##n, u32 f##n
__global__ void __launch_bounds__(576, 3)
adaln_block_kernel(D(0), D(1), D(2), D(3), D(4), D(5), D(6), D(7), D(8), D(9), D(10), D(11), D(12)) {
  __shared__ __attribute__((aligned(16))) char smem[98304];
  asm volatile(
".Lstart:\n"
"s_load_dwordx16 s[8:23], s[0:1], 0x0\n"
"s_load_dwordx16 s[24:39], s[0:1], 0x40\n"
"v_lshrrev_b32 v3, 6, v0\n"
"v_and_b32 v1, 63, v0\n"
"s_and_b32 s60, s2, 7\n"
"v_readfirstlane_b32 s3, v3\n"
"s_bfe_u32 s61, s2, 0x40003\n"
"v_lshlrev_b32 v2, 4, v1\n"
"s_mov_b32 s62, 20000\n"
"s_mov_b32 s67, 0xffffff00\n"
"s_bitcmp0_b32 s2, 7\n"
"s_cbranch_scc1 .LAUX0\n"
"s_lshr_b32 s4, s61, 3\n"
"s_lshl_b32 s66, s60, 1\n"
"s_add_u32 s66, s66, s4\n"
"s_lshl_b32 s65, s60, 4\n"
"s_add_u32 s65, s65, s61\n"
"s_cmp_eq_u32 s3, 8\n"
"s_cbranch_scc1 .LPOLLER\n"
"s_and_b32 s4, s61, 7\n"
"s_lshl_b32 s5, s66, 21\n"
"s_lshl_b32 s6, s4, 16\n"
"s_lshl_b32 s7, s3, 13\n"
"s_add_u32 s5, s5, s6\n"
"s_add_u32 s7, s7, 0x180000\n"
"s_add_u32 s6, s5, s7\n"
"s_add_u32 s7, s6, 4096\n"
"s_waitcnt lgkmcnt(0)\n"
"s_mov_b64 s[62:63], s[6:7]\n"
"s_getpc_b64 s[4:5]\n"
".LH0:\n"
"s_lshl_b32 s6, s3, 10\n"
"s_add_u32 s6, s6, .Lstart+1024-.LH0\n"
"s_ashr_i32 s7, s6, 31\n"
"s_add_u32 s4, s4, s6\n"
"s_addc_u32 s5, s5, s7\n"
"s_mov_b64 exec, 0\n"
"s_swappc_b64 vcc, s[4:5]\n"
"s_mov_b64 exec, -1\n"
"s_mov_b64 s[6:7], s[62:63]\n"
"s_mov_b32 s62, 20000\n"
"s_sleep 4\n"
"buffer_load_dwordx4 v[32:35], v2, s[8:11], s6 offen offset:0 nt\n"
"buffer_load_dwordx4 v[36:39], v2, s[8:11], s6 offen offset:1024 nt\n"
"buffer_load_dwordx4 v[40:43], v2, s[8:11], s6 offen offset:2048 nt\n"
"buffer_load_dwordx4 v[44:47], v2, s[8:11], s6 offen offset:3072 nt\n"
"buffer_load_dwordx4 v[48:51], v2, s[8:11], s7 offen offset:0 nt\n"
"buffer_load_dwordx4 v[52:55], v2, s[8:11], s7 offen offset:1024 nt\n"
"buffer_load_dwordx4 v[56:59], v2, s[8:11], s7 offen offset:2048 nt\n"
"buffer_load_dwordx4 v[60:63], v2, s[8:11], s7 offen offset:3072 nt\n"
"s_branch .LATT1\n"
".LRET:\n"
"s_setpc_b64 vcc\n"
".LAUX0:\n"
"v_lshrrev_b32 v3, 3, v1\n"
"v_and_b32 v5, 7, v1\n"
"v_lshlrev_b32 v4, 4, v5\n"
"s_and_b32 s65, s61, 1\n"
"s_lshl_b32 s66, s61, 2\n"
"s_and_b32 s66, s66, 56\n"
"s_add_u32 s66, s66, s60\n"
"s_cmp_gt_u32 s61, 8\n"
"s_cbranch_scc1 .LAUX1\n"
"s_cmp_lg_u32 s3, 0\n"
"s_cbranch_scc1 .LAUX1s\n"
"s_setprio 3\n"
"s_lshl_b32 s7, s61, 3\n"
"s_add_u32 s7, s7, s60\n"
"s_and_b32 s4, s7, 3\n"
"s_lshr_b32 s5, s7, 2\n"
"s_mul_i32 s6, s4, 995328\n"
"s_lshl_b32 s63, s5, 7\n"
"v_mul_u32_u24 v6, 18, v3\n"
"v_mov_b32 v12, 0\n"
"v_mov_b32 v13, 0\n"
"v_mov_b32 v14, 0\n"
"s_mul_i32 s64, s4, 288\n"
"v_add_lshl_u32 v7, v6, v5, 1\n"
"v_and_b32 v15, 1, v1\n"
"v_add_lshl_u32 v15, v6, v15, 1\n"
"v_mul_u32_u24 v6, 6912, v6\n"
"s_add_u32 s6, s6, s63\n"
"v_add_u32 v6, v6, v4\n"
"s_movk_i32 s63, 6912\n"
"s_waitcnt lgkmcnt(0)\n"
"buffer_load_short_d16_hi v12, v7, s[28:31], s64 offen\n"
"buffer_load_short_d16_hi v13, v7, s[28:31], s64 offen offset:16\n"
"buffer_load_short_d16_hi v14, v15, s[28:31], s64 offen offset:32\n"
"buffer_load_dwordx4 v[32:35], v6, s[24:27], s6 offen nt\n"
"s_add_u32 s6, s6, s63\n"
"buffer_load_dwordx4 v[36:39], v6, s[24:27], s6 offen nt\n"
"s_add_u32 s6, s6, s63\n"
"buffer_load_dwordx4 v[40:43], v6, s[24:27], s6 offen nt\n"
"s_add_u32 s6, s6, s63\n"
"buffer_load_dwordx4 v[44:47], v6, s[24:27], s6 offen nt\n"
"s_add_u32 s6, s6, s63\n"
"buffer_load_dwordx4 v[48:51], v6, s[24:27], s6 offen nt\n"
"s_add_u32 s6, s6, s63\n"
"buffer_load_dwordx4 v[52:55], v6, s[24:27], s6 offen nt\n"
"s_add_u32 s6, s6, s63\n"
"buffer_load_dwordx4 v[56:59], v6, s[24:27], s6 offen nt\n"
"s_add_u32 s6, s6, s63\n"
"buffer_load_dwordx4 v[60:63], v6, s[24:27], s6 offen nt\n"
"s_add_u32 s6, s6, s63\n"
"buffer_load_dwordx4 v[64:67], v6, s[24:27], s6 offen nt\n"
"s_add_u32 s6, s6, s63\n"
"buffer_load_dwordx4 v[68:71], v6, s[24:27], s6 offen nt\n"
"s_add_u32 s6, s6, s63\n"
"buffer_load_dwordx4 v[72:75], v6, s[24:27], s6 offen nt\n"
"s_add_u32 s6, s6, s63\n"
"buffer_load_dwordx4 v[76:79], v6, s[24:27], s6 offen nt\n"
"s_add_u32 s6, s6, s63\n"
"buffer_load_dwordx4 v[80:83], v6, s[24:27], s6 offen nt\n"
"s_add_u32 s6, s6, s63\n"
"buffer_load_dwordx4 v[84:87], v6, s[24:27], s6 offen nt\n"
"s_add_u32 s6, s6, s63\n"
"buffer_load_dwordx4 v[88:91], v6, s[24:27], s6 offen nt\n"
"s_add_u32 s6, s6, s63\n"
"buffer_load_dwordx4 v[92:95], v6, s[24:27], s6 offen nt\n"
"s_add_u32 s6, s6, s63\n"
"buffer_load_dwordx4 v[96:99], v6, s[24:27], s6 offen nt\n"
"s_add_u32 s6, s6, s63\n"
"buffer_load_dwordx4 v[100:103], v6, s[24:27], s6 offen nt\n"
"buffer_load_dwordx2 v[16:17], off, s[16:19], 0 sc1\n"
"s_branch .LS1\n"
".LAUX1s:\n"
"s_getpc_b64 s[4:5]\n"
".LH1:\n"
"s_lshl_b32 s6, s3, 10\n"
"s_add_u32 s6, s6, .Lstart+1024-.LH1\n"
"s_ashr_i32 s7, s6, 31\n"
"s_add_u32 s4, s4, s6\n"
"s_addc_u32 s5, s5, s7\n"
"s_mov_b64 exec, 0\n"
"s_swappc_b64 vcc, s[4:5]\n"
"s_cmp_ge_u32 s3, 6\n"
"s_cbranch_scc0 .LHE1\n"
"s_add_u32 s4, s4, 0xc00\n"
"s_addc_u32 s5, s5, 0\n"
"s_swappc_b64 vcc, s[4:5]\n"
".LHE1:\n"
"s_mov_b64 exec, -1\n"
"s_sleep 2\n"
".LAUX1:\n"
"s_waitcnt lgkmcnt(0)\n"
"buffer_load_dwordx2 v[16:17], off, s[16:19], 0 sc1\n"
".LAUX2:\n"
"v_and_b32 v6, 24, v1\n"
"v_lshrrev_b32 v7, 5, v1\n"
"v_lshl_or_b32 v6, v7, 2, v6\n"
"s_mul_i32 s4, s65, 288\n"
"s_lshl_b32 s5, s3, 5\n"
"v_lshl_or_b32 v6, v6, 13, v4\n"
"s_add_u32 s4, s4, s5\n"
"s_lshl_b32 s5, s4, 13\n"
"s_lshl_b32 s6, s66, 7\n"
"s_add_u32 s5, s5, s6\n"
"v_and_b32 v7, 31, v1\n"
"v_mov_b32 v18, 0\n"
"v_add_lshl_u32 v7, v7, s4, 1\n"
"buffer_load_dwordx4 v[120:123], v2, s[32:35], 0 offen\n"
"buffer_load_dwordx4 v[124:127], v2, s[32:35], 0 offen offset:1024\n"
"buffer_load_short_d16_hi v18, v7, s[32:35], 0 offen\n"
"buffer_load_dwordx4 v[104:107], v6, s[20:23], s5 offen nt\n"
"s_add_u32 s5, s5, 8192\n"
"buffer_load_dwordx4 v[108:111], v6, s[20:23], s5 offen nt\n"
"s_add_u32 s5, s5, 8192\n"
"buffer_load_dwordx4 v[112:115], v6, s[20:23], s5 offen nt\n"
"s_add_u32 s5, s5, 8192\n"
"buffer_load_dwordx4 v[116:119], v6, s[20:23], s5 offen nt\n"
"s_lshl_b32 s4, s60, 9\n"
"s_lshl_b32 s5, s3, 6\n"
"s_add_u32 s4, s4, s5\n"
"s_mul_i32 s8, s4, 1152\n"
"s_lshl_b32 s5, s61, 7\n"
"s_add_u32 s8, s8, s5\n"
"s_movk_i32 s9, 1152\n"
"s_cmp_eq_u32 s3, 8\n"
"s_cselect_b32 s38, 0, s38\n"
"s_cmp_gt_u32 s61, 8\n"
"s_cbranch_scc0 .LJOB\n"
"s_lshl_b32 s4, s61, 3\n"
"s_add_u32 s4, s4, s60\n"
"s_mov_b64 s[36:37], s[24:25]\n"
"s_cmp_gt_u32 s4, 107\n"
"s_cselect_b32 s38, 0, s26\n"
"s_mul_i32 s8, s3, 442368\n"
"s_lshl_b32 s4, s4, 7\n"
"s_add_u32 s8, s8, s4\n"
"s_sub_u32 s8, s8, 6912\n"
"s_movk_i32 s9, 6912\n"
".LJOB:\n"
"v_mad_u32_u24 v160, v3, s9, v4\n"
"s_lshl_b32 s9, s9, 3\n"
"s_branch .LAUX3\n"
".LS1:\n"
"s_load_dwordx16 s[40:55], s[0:1], 0x80\n"
"s_load_dwordx4 s[56:59], s[0:1], 0xc0\n"
"s_cbranch_execz .LRET\n"
"v_lshrrev_b32 v19, 1, v5\n"
"v_and_b32 v7, 1, v1\n"
"v_mov_b32 v11, 0xffff\n"
"v_lshlrev_b32 v7, 4, v7\n"
"v_lshlrev_b32 v7, v7, v11\n"
"v_cmp_eq_u32 vcc, 0, v19\n"
"s_nop 1\n"
"v_cndmask_b32 v8, 0, v7, vcc\n"
"v_cmp_eq_u32 vcc, 1, v19\n"
"s_nop 1\n"
"v_cndmask_b32 v9, 0, v7, vcc\n"
"v_cmp_eq_u32 vcc, 2, v19\n"
"s_nop 1\n"
"v_cndmask_b32 v10, 0, v7, vcc\n"
"v_cmp_eq_u32 vcc, 3, v19\n"
"s_nop 1\n"
"v_cndmask_b32 v11, 0, v7, vcc\n"
"v_bfe_u32 v6, v1, 4, 1\n"
"v_lshrrev_b32 v7, 5, v1\n"
"v_lshlrev_b32 v6, 11, v6\n"
"v_lshl_or_b32 v6, v7, 7, v6\n"
"v_or_b32 v6, v6, v4\n"
"s_and_b32 s4, s7, 3\n"
"s_lshr_b32 s5, s7, 2\n"
"s_cmp_gt_u32 s5, 8\n"
"s_cselect_b32 s0, 9, 0\n"
"s_cselect_b32 s1, 1024, 0\n"
"s_sub_u32 s5, s5, s0\n"
"s_lshl_b32 s5, s5, 12\n"
"s_lshl_b32 s4, s4, 8\n"
"s_add_u32 s5, s5, s1\n"
"s_add_u32 s5, s5, s4\n"
"s_add_u32 s5, s5, 4096\n"
"s_waitcnt vmcnt(19)\n"
"v_mul_f32 v24, 0xbfb8aa3b, v12\n"
"v_mul_f32 v25, 0xbfb8aa3b, v13\n"
"v_mul_f32 v26, 0xbfb8aa3b, v14\n"
"v_exp_f32 v24, v24\n"
"v_exp_f32 v25, v25\n"
"v_exp_f32 v26, v26\n"
"v_add_f32 v24, 1.0, v24\n"
"v_add_f32 v25, 1.0, v25\n"
"v_add_f32 v26, 1.0, v26\n"
"v_rcp_f32 v24, v24\n"
"v_rcp_f32 v25, v25\n"
"v_rcp_f32 v26, v26\n"
"v_mul_f32 v24, v12, v24\n"
"v_mul_f32 v25, v13, v25\n"
"v_mul_f32 v26, v14, v26\n"
"v_cvt_pk_bf16_f32 v12, v24, v24\n"
"v_cvt_pk_bf16_f32 v13, v24, v24\n"
"v_cvt_pk_bf16_f32 v14, v25, v25\n"
"v_cvt_pk_bf16_f32 v15, v25, v25\n"
"v_cvt_pk_bf16_f32 v19, v26, v26\n"
"v_mov_b32_dpp v12, v12 row_shr:4 row_mask:0xf bank_mask:0xa\n"
"v_mov_b32_dpp v13, v13 row_shl:4 row_mask:0xf bank_mask:0x5\n"
"v_mov_b32_dpp v14, v14 row_shr:4 row_mask:0xf bank_mask:0xa\n"
"v_mov_b32_dpp v15, v15 row_shl:4 row_mask:0xf bank_mask:0x5\n"
"v_and_b32_dpp v20, v12, v8 quad_perm:[0,0,0,0] row_mask:0xf bank_mask:0xf\n"
"v_and_b32_dpp v21, v12, v9 quad_perm:[0,0,0,0] row_mask:0xf bank_mask:0xf\n"
"v_and_b32_dpp v22, v12, v10 quad_perm:[0,0,0,0] row_mask:0xf bank_mask:0xf\n"
"v_and_b32_dpp v23, v12, v11 quad_perm:[0,0,0,0] row_mask:0xf bank_mask:0xf\n"
"v_and_b32_dpp v24, v12, v8 quad_perm:[1,1,1,1] row_mask:0xf bank_mask:0xf\n"
"v_and_b32_dpp v25, v12, v9 quad_perm:[1,1,1,1] row_mask:0xf bank_mask:0xf\n"
"v_and_b32_dpp v26, v12, v10 quad_perm:[1,1,1,1] row_mask:0xf bank_mask:0xf\n"
"v_and_b32_dpp v27, v12, v11 quad_perm:[1,1,1,1] row_mask:0xf bank_mask:0xf\n"
"s_waitcnt vmcnt(18)\n"
"v_mfma_f32_16x16x32_bf16 v[28:31], v[32:35], v[20:23], 0\n"
"v_and_b32_dpp v20, v12, v8 quad_perm:[2,2,2,2] row_mask:0xf bank_mask:0xf\n"
"v_and_b32_dpp v21, v12, v9 quad_perm:[2,2,2,2] row_mask:0xf bank_mask:0xf\n"
"v_and_b32_dpp v22, v12, v10 quad_perm:[2,2,2,2] row_mask:0xf bank_mask:0xf\n"
"v_and_b32_dpp v23, v12, v11 quad_perm:[2,2,2,2] row_mask:0xf bank_mask:0xf\n"
"s_waitcnt vmcnt(17)\n"
"v_mfma_f32_16x16x32_bf16 v[28:31], v[36:39], v[24:27], v[28:31]\n"
"v_and_b32_dpp v24, v12, v8 quad_perm:[3,3,3,3] row_mask:0xf bank_mask:0xf\n"
"v_and_b32_dpp v25, v12, v9 quad_perm:[3,3,3,3] row_mask:0xf bank_mask:0xf\n"
"v_and_b32_dpp v26, v12, v10 quad_perm:[3,3,3,3] row_mask:0xf bank_mask:0xf\n"
"v_and_b32_dpp v27, v12, v11 quad_perm:[3,3,3,3] row_mask:0xf bank_mask:0xf\n"
"s_waitcnt vmcnt(16)\n"
"v_mfma_f32_16x16x32_bf16 v[28:31], v[40:43], v[20:23], v[28:31]\n"
"v_and_b32_dpp v20, v13, v8 quad_perm:[0,0,0,0] row_mask:0xf bank_mask:0xf\n"
"v_and_b32_dpp v21, v13, v9 quad_perm:[0,0,0,0] row_mask:0xf bank_mask:0xf\n"
"v_and_b32_dpp v22, v13, v10 quad_perm:[0,0,0,0] row_mask:0xf bank_mask:0xf\n"
"v_and_b32_dpp v23, v13, v11 quad_perm:[0,0,0,0] row_mask:0xf bank_mask:0xf\n"
"s_waitcnt vmcnt(15)\n"
"v_mfma_f32_16x16x32_bf16 v[28:31], v[44:47], v[24:27], v[28:31]\n"
"v_and_b32_dpp v24, v13, v8 quad_perm:[1,1,1,1] row_mask:0xf bank_mask:0xf\n"
"v_and_b32_dpp v25, v13, v9 quad_perm:[1,1,1,1] row_mask:0xf bank_mask:0xf\n"
"v_and_b32_dpp v26, v13, v10 quad_perm:[1,1,1,1] row_mask:0xf bank_mask:0xf\n"
"v_and_b32_dpp v27, v13, v11 quad_perm:[1,1,1,1] row_mask:0xf bank_mask:0xf\n"
"s_waitcnt vmcnt(14)\n"
"v_mfma_f32_16x16x32_bf16 v[28:31], v[48:51], v[20:23], v[28:31]\n"
"v_and_b32_dpp v20, v13, v8 quad_perm:[2,2,2,2] row_mask:0xf bank_mask:0xf\n"
"v_and_b32_dpp v21, v13, v9 quad_perm:[2,2,2,2] row_mask:0xf bank_mask:0xf\n"
"v_and_b32_dpp v22, v13, v10 quad_perm:[2,2,2,2] row_mask:0xf bank_mask:0xf\n"
"v_and_b32_dpp v23, v13, v11 quad_perm:[2,2,2,2] row_mask:0xf bank_mask:0xf\n"
"s_waitcnt vmcnt(13)\n"
"v_mfma_f32_16x16x32_bf16 v[28:31], v[52:55], v[24:27], v[28:31]\n"
"v_and_b32_dpp v24, v13, v8 quad_perm:[3,3,3,3] row_mask:0xf bank_mask:0xf\n"
"v_and_b32_dpp v25, v13, v9 quad_perm:[3,3,3,3] row_mask:0xf bank_mask:0xf\n"
"v_and_b32_dpp v26, v13, v10 quad_perm:[3,3,3,3] row_mask:0xf bank_mask:0xf\n"
"v_and_b32_dpp v27, v13, v11 quad_perm:[3,3,3,3] row_mask:0xf bank_mask:0xf\n"
"s_waitcnt vmcnt(12)\n"
"v_mfma_f32_16x16x32_bf16 v[28:31], v[56:59], v[20:23], v[28:31]\n"
"v_and_b32_dpp v20, v14, v8 quad_perm:[0,0,0,0] row_mask:0xf bank_mask:0xf\n"
"v_and_b32_dpp v21, v14, v9 quad_perm:[0,0,0,0] row_mask:0xf bank_mask:0xf\n"
"v_and_b32_dpp v22, v14, v10 quad_perm:[0,0,0,0] row_mask:0xf bank_mask:0xf\n"
"v_and_b32_dpp v23, v14, v11 quad_perm:[0,0,0,0] row_mask:0xf bank_mask:0xf\n"
"s_waitcnt vmcnt(11)\n"
"v_mfma_f32_16x16x32_bf16 v[28:31], v[60:63], v[24:27], v[28:31]\n"
"v_and_b32_dpp v24, v14, v8 quad_perm:[1,1,1,1] row_mask:0xf bank_mask:0xf\n"
"v_and_b32_dpp v25, v14, v9 quad_perm:[1,1,1,1] row_mask:0xf bank_mask:0xf\n"
"v_and_b32_dpp v26, v14, v10 quad_perm:[1,1,1,1] row_mask:0xf bank_mask:0xf\n"
"v_and_b32_dpp v27, v14, v11 quad_perm:[1,1,1,1] row_mask:0xf bank_mask:0xf\n"
"s_waitcnt vmcnt(10)\n"
"v_mfma_f32_16x16x32_bf16 v[28:31], v[64:67], v[20:23], v[28:31]\n"
"v_and_b32_dpp v20, v14, v8 quad_perm:[2,2,2,2] row_mask:0xf bank_mask:0xf\n"
"v_and_b32_dpp v21, v14, v9 quad_perm:[2,2,2,2] row_mask:0xf bank_mask:0xf\n"
"v_and_b32_dpp v22, v14, v10 quad_perm:[2,2,2,2] row_mask:0xf bank_mask:0xf\n"
"v_and_b32_dpp v23, v14, v11 quad_perm:[2,2,2,2] row_mask:0xf bank_mask:0xf\n"
"s_waitcnt vmcnt(9)\n"
"v_mfma_f32_16x16x32_bf16 v[28:31], v[68:71], v[24:27], v[28:31]\n"
"v_and_b32_dpp v24, v14, v8 quad_perm:[3,3,3,3] row_mask:0xf bank_mask:0xf\n"
"v_and_b32_dpp v25, v14, v9 quad_perm:[3,3,3,3] row_mask:0xf bank_mask:0xf\n"
"v_and_b32_dpp v26, v14, v10 quad_perm:[3,3,3,3] row_mask:0xf bank_mask:0xf\n"
"v_and_b32_dpp v27, v14, v11 quad_perm:[3,3,3,3] row_mask:0xf bank_mask:0xf\n"
"s_waitcnt vmcnt(8)\n"
"v_mfma_f32_16x16x32_bf16 v[28:31], v[72:75], v[20:23], v[28:31]\n"
"v_and_b32_dpp v20, v15, v8 quad_perm:[0,0,0,0] row_mask:0xf bank_mask:0xf\n"
"v_and_b32_dpp v21, v15, v9 quad_perm:[0,0,0,0] row_mask:0xf bank_mask:0xf\n"
"v_and_b32_dpp v22, v15, v10 quad_perm:[0,0,0,0] row_mask:0xf bank_mask:0xf\n"
"v_and_b32_dpp v23, v15, v11 quad_perm:[0,0,0,0] row_mask:0xf bank_mask:0xf\n"
"s_waitcnt vmcnt(7)\n"
"v_mfma_f32_16x16x32_bf16 v[28:31], v[76:79], v[24:27], v[28:31]\n"
"v_and_b32_dpp v24, v15, v8 quad_perm:[1,1,1,1] row_mask:0xf bank_mask:0xf\n"
"v_and_b32_dpp v25, v15, v9 quad_perm:[1,1,1,1] row_mask:0xf bank_mask:0xf\n"
"v_and_b32_dpp v26, v15, v10 quad_perm:[1,1,1,1] row_mask:0xf bank_mask:0xf\n"
"v_and_b32_dpp v27, v15, v11 quad_perm:[1,1,1,1] row_mask:0xf bank_mask:0xf\n"
"s_waitcnt vmcnt(6)\n"
"v_mfma_f32_16x16x32_bf16 v[28:31], v[80:83], v[20:23], v[28:31]\n"
"v_and_b32_dpp v20, v15, v8 quad_perm:[2,2,2,2] row_mask:0xf bank_mask:0xf\n"
"v_and_b32_dpp v21, v15, v9 quad_perm:[2,2,2,2] row_mask:0xf bank_mask:0xf\n"
"v_and_b32_dpp v22, v15, v10 quad_perm:[2,2,2,2] row_mask:0xf bank_mask:0xf\n"
"v_and_b32_dpp v23, v15, v11 quad_perm:[2,2,2,2] row_mask:0xf bank_mask:0xf\n"
"s_waitcnt vmcnt(5)\n"
"v_mfma_f32_16x16x32_bf16 v[28:31], v[84:87], v[24:27], v[28:31]\n"
"v_and_b32_dpp v24, v15, v8 quad_perm:[3,3,3,3] row_mask:0xf bank_mask:0xf\n"
"v_and_b32_dpp v25, v15, v9 quad_perm:[3,3,3,3] row_mask:0xf bank_mask:0xf\n"
"v_and_b32_dpp v26, v15, v10 quad_perm:[3,3,3,3] row_mask:0xf bank_mask:0xf\n"
"v_and_b32_dpp v27, v15, v11 quad_perm:[3,3,3,3] row_mask:0xf bank_mask:0xf\n"
"s_waitcnt vmcnt(4)\n"
"v_mfma_f32_16x16x32_bf16 v[28:31], v[88:91], v[20:23], v[28:31]\n"
"v_and_b32_dpp v20, v19, v8 quad_perm:[0,0,0,0] row_mask:0xf bank_mask:0xf\n"
"v_and_b32_dpp v21, v19, v9 quad_perm:[0,0,0,0] row_mask:0xf bank_mask:0xf\n"
"v_and_b32_dpp v22, v19, v10 quad_perm:[0,0,0,0] row_mask:0xf bank_mask:0xf\n"
"s_nop 0\n"
"s_cbranch_execz .LRET\n"
"v_and_b32_dpp v23, v19, v11 quad_perm:[0,0,0,0] row_mask:0xf bank_mask:0xf\n"
"s_waitcnt vmcnt(3)\n"
"v_mfma_f32_16x16x32_bf16 v[28:31], v[92:95], v[24:27], v[28:31]\n"
"v_and_b32_dpp v24, v19, v8 quad_perm:[1,1,1,1] row_mask:0xf bank_mask:0xf\n"
"v_and_b32_dpp v25, v19, v9 quad_perm:[1,1,1,1] row_mask:0xf bank_mask:0xf\n"
"v_and_b32_dpp v26, v19, v10 quad_perm:[1,1,1,1] row_mask:0xf bank_mask:0xf\n"
"v_and_b32_dpp v27, v19, v11 quad_perm:[1,1,1,1] row_mask:0xf bank_mask:0xf\n"
"s_waitcnt vmcnt(2)\n"
"v_mfma_f32_16x16x32_bf16 v[28:31], v[96:99], v[20:23], v[28:31]\n"
"s_waitcnt vmcnt(1)\n"
"v_mfma_f32_16x16x32_bf16 v[28:31], v[100:103], v[24:27], v[28:31]\n"
"s_waitcnt vmcnt(0)\n"
"v_readfirstlane_b32 s63, v16\n"
"v_readfirstlane_b32 s64, v17\n"
"s_add_u32 s63, s63, 1\n"
"s_addc_u32 s64, s64, 0\n"
"s_and_b32 s64, s64, 0xff\n"
"v_mov_b32 v20, s63\n"
"v_mov_b32 v22, s63\n"
"v_mov_b32 v24, s64\n"
"v_mov_b32_dpp v28, v28 row_ror:8 row_mask:0xc bank_mask:0xf\n"
"v_mov_b32_dpp v29, v29 row_ror:8 row_mask:0xc bank_mask:0xf\n"
"v_mov_b32_dpp v30, v30 row_ror:8 row_mask:0xc bank_mask:0xf\n"
"v_mov_b32_dpp v31, v31 row_ror:8 row_mask:0xc bank_mask:0xf\n"
"v_permlane32_swap_b32 v28, v29\n"
"s_nop 0\n"
"v_permlane32_swap_b32 v30, v31\n"
"v_add_f32 v28, v28, v29\n"
"v_add_f32 v30, v30, v31\n"
"v_bfi_b32 v21, s67, v28, v24\n"
"v_bfi_b32 v23, s67, v30, v24\n"
"s_mov_b32 exec_lo, 0xff00ff\n"
"s_mov_b32 exec_hi, 0xff00ff\n"
"buffer_store_dwordx4 v[20:23], v6, s[16:19], s5 offen sc1\n"
"s_mov_b64 exec, -1\n"
"s_setprio 0\n"
"s_branch .LAUX2\n"
".LATT1:\n"
"s_load_dwordx16 s[40:55], s[0:1], 0x80\n"
"s_load_dwordx4 s[56:59], s[0:1], 0xc0\n"
"s_barrier\n"
"s_mul_i32 s5, s3, 9216\n"
"s_add_u32 s5, s5, 20112\n"
"v_bfe_u32 v3, v1, 2, 2\n"
"v_lshrrev_b32 v5, 4, v1\n"
"v_and_b32 v6, 3, v1\n"
"v_and_b32 v7, 15, v1\n"
"v_mul_u32_u24 v8, 272, v5\n"
"v_lshl_add_u32 v10, v5, 3, v3\n"
"v_lshl_add_u32 v8, v7, 4, v8\n"
"v_mul_u32_u24 v10, 272, v10\n"
"v_add_u32 v8, s5, v8\n"
"v_lshl_add_u32 v10, v6, 3, v10\n"
"v_add_u32 v10, s5, v10\n"
"v_mul_u32_u24 v4, 272, v7\n"
"v_lshl_add_u32 v4, v5, 4, v4\n"
"v_add_u32 v4, s5, v4\n"
"v_bfe_u32 v11, v1, 2, 1\n"
"v_lshlrev_b32 v12, 4, v5\n"
"v_lshl_or_b32 v12, v11, 8, v12\n"
"v_add_u32 v12, 5120, v12\n"
"v_mov_b32 v13, 5632\n"
"v_cmp_gt_u32 vcc, 8, v7\n"
"v_lshrrev_b32 v14, 5, v1\n"
"v_lshlrev_b32 v9, 9, v14\n"
"v_cndmask_b32 v28, v13, v12, vcc\n"
"v_cndmask_b32 v29, v12, v13, vcc\n"
"v_lshl_add_u32 v9, v7, 2, v9\n"
"v_add_u32 v9, s5, v9\n"
"s_lshr_b32 s4, s3, 1\n"
"v_mov_b32 v30, s4\n"
"s_cmp_lt_u32 s3, 4\n"
"s_cselect_b64 vcc, -1, 0\n"
"v_and_b32 v31, 255, v0\n"
"v_cndmask_b32 v30, v14, v30, vcc\n"
"v_lshlrev_b32 v31, 2, v31\n"
"v_lshlrev_b32 v30, 6, v30\n"
"v_lshl_add_u32 v30, v7, 2, v30\n"
"v_add_u32 v30, 5904, v30\n"
"v_add_u32 v11, 20112, v31\n"
"v_bfe_u32 v3, v1, 4, 1\n"
"s_lshl_b32 s4, s3, 2\n"
"s_add_u32 s4, s4, 5904\n"
"v_lshl_add_u32 v3, v3, 6, s4\n"
"s_and_b32 s4, s61, 7\n"
"s_lshl_b32 s0, s66, 3\n"
"s_add_u32 s0, s0, s4\n"
"s_mul_i32 s0, s0, 1152\n"
"s_add_u32 s0, s0, 131072\n"
"s_waitcnt vmcnt(7)\n"
"ds_write_b128 v8, v[32:35] offset:0\n"
"buffer_load_dwordx4 v[64:67], v2, s[12:15], s6 offen offset:0 nt\n"
"s_waitcnt vmcnt(7)\n"
"ds_write_b128 v8, v[36:39] offset:1088\n"
"buffer_load_dwordx4 v[68:71], v2, s[12:15], s6 offen offset:1024 nt\n"
"s_waitcnt vmcnt(7)\n"
"ds_write_b128 v8, v[40:43] offset:2176\n"
"buffer_load_dwordx4 v[72:75], v2, s[12:15], s6 offen offset:2048 nt\n"
"s_waitcnt vmcnt(7)\n"
"ds_write_b128 v8, v[44:47] offset:3264\n"
"buffer_load_dwordx4 v[76:79], v2, s[12:15], s6 offen offset:3072 nt\n"
"s_waitcnt vmcnt(7)\n"
"ds_write_b128 v8, v[48:51] offset:4352\n"
"buffer_load_dwordx4 v[80:83], v2, s[12:15], s7 offen offset:0 nt\n"
"s_waitcnt vmcnt(7)\n"
"ds_write_b128 v8, v[52:55] offset:5440\n"
"buffer_load_dwordx4 v[84:87], v2, s[12:15], s7 offen offset:1024 nt\n"
"s_waitcnt vmcnt(7)\n"
"ds_write_b128 v8, v[56:59] offset:6528\n"
"buffer_load_dwordx4 v[88:91], v2, s[12:15], s7 offen offset:2048 nt\n"
"s_waitcnt vmcnt(7)\n"
"ds_write_b128 v8, v[60:63] offset:7616\n"
"buffer_load_dwordx4 v[92:95], v2, s[12:15], s7 offen offset:3072 nt\n"
"ds_read_b64_tr_b16 v[32:33], v10 offset:0\n"
"ds_read_b64_tr_b16 v[34:35], v10 offset:1088\n"
"ds_read_b64_tr_b16 v[36:37], v10 offset:32\n"
"ds_read_b64_tr_b16 v[38:39], v10 offset:1120\n"
"ds_read_b64_tr_b16 v[40:41], v10 offset:64\n"
"ds_read_b64_tr_b16 v[42:43], v10 offset:1152\n"
"ds_read_b64_tr_b16 v[44:45], v10 offset:96\n"
"ds_read_b64_tr_b16 v[46:47], v10 offset:1184\n"
"ds_read_b64_tr_b16 v[48:49], v10 offset:128\n"
"ds_read_b64_tr_b16 v[50:51], v10 offset:1216\n"
"ds_read_b64_tr_b16 v[52:53], v10 offset:160\n"
"ds_read_b64_tr_b16 v[54:55], v10 offset:1248\n"
"ds_read_b64_tr_b16 v[56:57], v10 offset:192\n"
"ds_read_b64_tr_b16 v[58:59], v10 offset:1280\n"
"ds_read_b64_tr_b16 v[60:61], v10 offset:224\n"
"ds_read_b64_tr_b16 v[62:63], v10 offset:1312\n"
"s_waitcnt vmcnt(7)\n"
"ds_write_b128 v8, v[64:67] offset:0\n"
"s_waitcnt vmcnt(6)\n"
"ds_write_b128 v8, v[68:71] offset:1088\n"
"s_waitcnt vmcnt(5)\n"
"ds_write_b128 v8, v[72:75] offset:2176\n"
"s_waitcnt vmcnt(4)\n"
"ds_write_b128 v8, v[76:79] offset:3264\n"
"ds_read_b128 v[64:67], v4 offset:0\n"
"ds_read_b128 v[68:71], v4 offset:64\n"
"ds_read_b128 v[72:75], v4 offset:128\n"
"ds_read_b128 v[76:79], v4 offset:192\n"
"ds_read_b32 v16, v13 offset:256\n"
"ds_read_b128 v[96:99], v28 offset:0\n"
"ds_read_b128 v[100:103], v28 offset:64\n"
"ds_read_b128 v[104:107], v28 offset:128\n"
"ds_read_b128 v[108:111], v28 offset:192\n"
"ds_read_b128 v[112:115], v29 offset:0\n"
"ds_read_b128 v[116:119], v29 offset:64\n"
"ds_read_b128 v[120:123], v29 offset:128\n"
"ds_read_b128 v[124:127], v29 offset:192\n"
"s_waitcnt lgkmcnt(8)\n"
"v_readfirstlane_b32 s1, v16\n"
"s_waitcnt vmcnt(3)\n"
"ds_write_b128 v8, v[80:83] offset:4352\n"
"s_waitcnt vmcnt(2)\n"
"ds_write_b128 v8, v[84:87] offset:5440\n"
"s_waitcnt vmcnt(1)\n"
"ds_write_b128 v8, v[88:91] offset:6528\n"
"s_waitcnt vmcnt(0)\n"
"s_nop 0\n"
"s_cbranch_execz .LRET\n"
"ds_write_b128 v8, v[92:95] offset:7616\n"
"ds_read_b128 v[80:83], v4 offset:4352\n"
"ds_read_b128 v[84:87], v4 offset:4416\n"
"ds_read_b128 v[88:91], v4 offset:4480\n"
"ds_read_b128 v[92:95], v4 offset:4544\n"
"s_cmp_eq_u32 s1, 1\n"
"s_cbranch_scc1 .LD_f\n"
"s_branch .LR_f\n"
".LP_f:\n"
"ds_read_b32 v16, v13 offset:256\n"
"ds_read_b128 v[96:99], v28 offset:0\n"
"ds_read_b128 v[100:103], v28 offset:64\n"
"ds_read_b128 v[104:107], v28 offset:128\n"
"ds_read_b128 v[108:111], v28 offset:192\n"
"ds_read_b128 v[112:115], v29 offset:0\n"
"ds_read_b128 v[116:119], v29 offset:64\n"
"ds_read_b128 v[120:123], v29 offset:128\n"
"ds_read_b128 v[124:127], v29 offset:192\n"
"s_waitcnt lgkmcnt(8)\n"
"v_readfirstlane_b32 s1, v16\n"
"s_cmp_lg_u32 s1, 1\n"
"s_cbranch_scc1 .LR_f\n"
".LD_f:\n"
"s_waitcnt lgkmcnt(0)\n"
"v_mfma_f32_16x16x32_bf16 v[12:15], v[96:99], v[64:67], 0\n"
"v_mfma_f32_16x16x32_bf16 v[12:15], v[100:103], v[68:71], v[12:15]\n"
"v_mfma_f32_16x16x32_bf16 v[12:15], v[104:107], v[72:75], v[12:15]\n"
"v_mfma_f32_16x16x32_bf16 v[12:15], v[108:111], v[76:79], v[12:15]\n"
"v_mfma_f32_16x16x32_bf16 v[12:15], v[112:115], v[80:83], v[12:15]\n"
"v_mfma_f32_16x16x32_bf16 v[12:15], v[116:119], v[84:87], v[12:15]\n"
"v_mfma_f32_16x16x32_bf16 v[12:15], v[120:123], v[88:91], v[12:15]\n"
"v_mfma_f32_16x16x32_bf16 v[12:15], v[124:127], v[92:95], v[12:15]\n"
"s_mov_b32 s4, 0x3fb8aa3b\n"
"s_nop 6\n"
"v_cvt_pk_bf16_f32 v20, v12, v12\n"
"v_lshlrev_b32 v20, 16, v20\n"
"v_mul_f32 v20, 0x3db504f3, v20\n"
"v_cvt_pk_bf16_f32 v20, v20, v20\n"
"v_lshlrev_b32 v20, 16, v20\n"
"s_nop 1\n"
"v_max_f32_dpp v24, v20, v20 quad_perm:[1,0,3,2] row_mask:0xf bank_mask:0xf\n"
"s_nop 1\n"
"v_max_f32_dpp v24, v24, v24 quad_perm:[2,3,0,1] row_mask:0xf bank_mask:0xf\n"
"s_nop 1\n"
"v_max_f32_dpp v24, v24, v24 row_half_mirror row_mask:0xf bank_mask:0xf\n"
"s_nop 1\n"
"v_max_f32_dpp v24, v24, v24 row_ror:8 row_mask:0xf bank_mask:0xf\n"
"v_mov_b32 v25, v24\n"
"s_nop 1\n"
"v_permlane32_swap_b32 v24, v25\n"
"v_max_f32 v24, v24, v25\n"
"v_mul_f32 v26, s4, v24\n"
"v_fma_f32 v20, v20, s4, -v26\n"
"v_exp_f32 v27, v20\n"
"s_nop 1\n"
"v_mov_b32_dpp v21, v27 row_shl:1 row_mask:0xf bank_mask:0xf bound_ctrl:0\n"
"v_cvt_pk_bf16_f32 v16, v27, v21\n"
"v_mov_b32 v21, v16\n"
"v_add_f32_dpp v27, v27, v27 quad_perm:[1,0,3,2] row_mask:0xf bank_mask:0xf\n"
"s_nop 0\n"
"v_permlane16_swap_b32 v16, v21\n"
"v_add_f32_dpp v27, v27, v27 quad_perm:[2,3,0,1] row_mask:0xf bank_mask:0xf\n"
"s_nop 0\n"
"v_mov_b32_dpp v16, v16 row_ror:8 row_mask:0xa bank_mask:0x3\n"
"v_mov_b32_dpp v16, v21 quad_perm:[0,1,2,3] row_mask:0xa bank_mask:0xc\n"
"v_mov_b32_dpp v16, v21 row_ror:8 row_mask:0x5 bank_mask:0xc\n"
"v_add_f32_dpp v27, v27, v27 row_half_mirror row_mask:0xf bank_mask:0xf\n"
"s_nop 0\n"
"v_mov_b32_dpp v17, v16 row_shl:2 row_mask:0xf bank_mask:0xf bound_ctrl:0\n"
"v_mov_b32_dpp v18, v16 row_shl:4 row_mask:0xf bank_mask:0xf bound_ctrl:0\n"
"v_mov_b32_dpp v19, v16 row_shl:6 row_mask:0xf bank_mask:0xf bound_ctrl:0\n"
"v_add_f32_dpp v27, v27, v27 row_ror:8 row_mask:0xf bank_mask:0xf\n"
"s_nop 0\n"
"v_mfma_f32_16x16x32_bf16 v[64:67], v[16:19], v[32:35], 0\n"
"v_mov_b32 v25, v27\n"
"v_mfma_f32_16x16x32_bf16 v[68:71], v[16:19], v[36:39], 0\n"
"v_mfma_f32_16x16x32_bf16 v[72:75], v[16:19], v[40:43], 0\n"
"v_permlane32_swap_b32 v27, v25\n"
"v_add_f32 v27, v27, v25\n"
"v_mfma_f32_16x16x32_bf16 v[76:79], v[16:19], v[44:47], 0\n"
"v_mfma_f32_16x16x32_bf16 v[80:83], v[16:19], v[48:51], 0\n"
"v_mfma_f32_16x16x32_bf16 v[84:87], v[16:19], v[52:55], 0\n"
"v_mfma_f32_16x16x32_bf16 v[88:91], v[16:19], v[56:59], 0\n"
"v_mfma_f32_16x16x32_bf16 v[92:95], v[16:19], v[60:63], 0\n"
"s_mov_b64 exec, 0x10001\n"
"ds_write2_b32 v3, v26, v27 offset1:8\n"
"s_mov_b32 exec_lo, 0xffff\n"
"s_mov_b32 exec_hi, 0xffff\n"
"ds_write2_b32 v9, v64, v68 offset0:0 offset1:16\n"
"ds_write2_b32 v9, v72, v76 offset0:32 offset1:48\n"
"ds_write2_b32 v9, v80, v84 offset0:64 offset1:80\n"
"s_nop 0\n"
"ds_write2_b32 v9, v88, v92 offset0:96 offset1:112\n"
"s_mov_b64 exec, -1\n"
"s_waitcnt lgkmcnt(0)\n"
"s_barrier\n"
"s_cmp_gt_u32 s3, 4\n"
"s_cbranch_scc1 .LM2E\n"
"ds_read_b32 v12, v30\n"
"ds_read2st64_b32 v[14:15], v11 offset0:0 offset1:36\n"
"ds_read2st64_b32 v[16:17], v11 offset0:72 offset1:108\n"
"ds_read2st64_b32 v[18:19], v11 offset0:144 offset1:180\n"
"ds_read2st64_b32 v[20:21], v11 offset0:216 offset1:252\n"
"s_waitcnt lgkmcnt(4)\n"
"v_max_f32_dpp v13, v12, v12 quad_perm:[1,0,3,2] row_mask:0xf bank_mask:0xf\n"
"s_nop 1\n"
"v_max_f32_dpp v13, v13, v13 quad_perm:[2,3,0,1] row_mask:0xf bank_mask:0xf\n"
"s_nop 1\n"
"v_max_f32_dpp v13, v13, v13 row_half_mirror row_mask:0xf bank_mask:0xf\n"
"v_sub_f32 v22, v12, v13\n"
"v_exp_f32 v22, v22\n"
"s_cmp_eq_u32 s3, 4\n"
"s_cbranch_scc1 .LATTML\n"
"s_waitcnt lgkmcnt(0)\n"
"v_mul_f32_dpp v24, v22, v14 row_newbcast:0 row_mask:0xf bank_mask:0xf\n"
"v_mul_f32_dpp v25, v22, v15 row_newbcast:1 row_mask:0xf bank_mask:0xf\n"
"v_fmac_f32_dpp v24, v22, v16 row_newbcast:2 row_mask:0xf bank_mask:0xf\n"
"v_fmac_f32_dpp v25, v22, v17 row_newbcast:3 row_mask:0xf bank_mask:0xf\n"
"v_fmac_f32_dpp v24, v22, v18 row_newbcast:4 row_mask:0xf bank_mask:0xf\n"
"v_fmac_f32_dpp v25, v22, v19 row_newbcast:5 row_mask:0xf bank_mask:0xf\n"
"v_fmac_f32_dpp v24, v22, v20 row_newbcast:6 row_mask:0xf bank_mask:0xf\n"
"v_fmac_f32_dpp v25, v22, v21 row_newbcast:7 row_mask:0xf bank_mask:0xf\n"
"v_add_f32 v24, v24, v25\n"
"v_min_u32 v24, -2, v24\n"
"s_nop 1\n"
"v_mov_b32_dpp v25, v24 quad_perm:[1,1,1,1] row_mask:0xf bank_mask:0xf\n"
"v_mov_b32_dpp v26, v24 quad_perm:[2,2,2,2] row_mask:0xf bank_mask:0xf\n"
"v_mov_b32_dpp v27, v24 quad_perm:[3,3,3,3] row_mask:0xf bank_mask:0xf\n"
"s_mov_b32 exec_lo, 0x11111111\n"
"s_mov_b32 exec_hi, 0x11111111\n"
"buffer_store_dwordx4 v[24:27], v31, s[16:19], s0 offen sc1\n"
"s_mov_b64 exec, -1\n"
"s_branch .LM2E\n"
".LATTML:\n"
"v_mul_f32_dpp v25, v12, v22 row_ror:8 row_mask:0xf bank_mask:0xf\n"
"v_min_u32 v24, -2, v13\n"
"s_nop 0\n"
"v_add_f32_dpp v25, v25, v25 quad_perm:[1,0,3,2] row_mask:0xf bank_mask:0xf\n"
"v_mov_b32 v26, v24\n"
"s_nop 0\n"
"v_add_f32_dpp v25, v25, v25 quad_perm:[2,3,0,1] row_mask:0xf bank_mask:0xf\n"
"v_mov_b32 v28, v24\n"
"s_nop 0\n"
"v_add_f32_dpp v25, v25, v25 row_half_mirror row_mask:0xf bank_mask:0xf\n"
"v_min_u32 v25, -2, v25\n"
"v_permlane32_swap_b32 v28, v26\n"
"v_mov_b32 v27, v25\n"
"v_mov_b32 v28, v25\n"
"s_nop 1\n"
"v_permlane32_swap_b32 v28, v27\n"
"s_mov_b64 exec, 1\n"
"buffer_store_dwordx4 v[24:27], off, s[16:19], s0 offset:1024 sc1\n"
"s_mov_b64 exec, -1\n"
"s_branch .LM2E\n"
".LPOLLER:\n"
"s_waitcnt lgkmcnt(0)\n"
"buffer_load_dwordx2 v[16:17], off, s[16:19], 0 sc1\n"
"s_load_dwordx16 s[40:55], s[0:1], 0x80\n"
"s_load_dwordx4 s[56:59], s[0:1], 0xc0\n"
"v_mov_b32 v8, 0\n"
"v_lshlrev_b32 v6, 2, v1\n"
"ds_write_b32 v6, v8 offset:5632\n"
"ds_write_b32 v8, v8 offset:5888\n"
"ds_write_b32 v8, v8 offset:5892\n"
"s_waitcnt lgkmcnt(0)\n"
"s_barrier\n"
"s_getpc_b64 s[4:5]\n"
".LH2:\n"
"s_cbranch_execz .LRET\n"
"s_lshl_b32 s6, s3, 10\n"
"s_add_u32 s6, s6, .Lstart+1024-.LH2\n"
"s_ashr_i32 s7, s6, 31\n"
"s_add_u32 s4, s4, s6\n"
"s_addc_u32 s5, s5, s7\n"
"s_mov_b64 exec, 0\n"
"s_swappc_b64 vcc, s[4:5]\n"
"s_add_u32 s4, s4, 0x400\n"
"s_addc_u32 s5, s5, 0\n"
"s_swappc_b64 vcc, s[4:5]\n"
"s_add_u32 s4, s4, 0x400\n"
"s_addc_u32 s5, s5, 0\n"
"s_swappc_b64 vcc, s[4:5]\n"
"s_add_u32 s4, s4, 0x400\n"
"s_addc_u32 s5, s5, 0\n"
"s_swappc_b64 vcc, s[4:5]\n"
"s_mov_b64 exec, -1\n"
"v_lshrrev_b32 v3, 5, v1\n"
"v_and_b32 v4, 3, v1\n"
"v_lshl_or_b32 v3, v4, 1, v3\n"
"v_and_b32 v4, 4, v1\n"
"v_lshl_or_b32 v3, v4, 3, v3\n"
"v_and_b32 v4, 8, v1\n"
"v_lshl_or_b32 v3, v4, 1, v3\n"
"v_and_b32 v4, 16, v1\n"
"v_lshrrev_b32 v4, 1, v4\n"
"v_or_b32 v3, v3, v4\n"
"v_lshlrev_b32 v3, 1, v3\n"
"v_add_u32 v5, 5120, v3\n"
"v_mov_b32 v9, 1\n"
"s_lshl_b32 s6, s66, 12\n"
"s_add_u32 s6, s6, 40960\n"
"s_setprio 3\n"
"s_waitcnt vmcnt(0)\n"
"v_readfirstlane_b32 s63, v16\n"
"v_readfirstlane_b32 s64, v17\n"
"s_add_u32 s63, s63, 1\n"
"s_addc_u32 s64, s64, 0\n"
"s_and_b32 s64, s64, 0xff\n"
".LP_q:\n"
"buffer_load_dwordx4 v[48:51], v2, s[16:19], s6 offen offset:0 sc1\n"
"buffer_load_dwordx4 v[52:55], v2, s[16:19], s6 offen offset:1024 sc1\n"
"buffer_load_dwordx4 v[56:59], v2, s[16:19], s6 offen offset:2048 sc1\n"
"buffer_load_dwordx4 v[60:63], v2, s[16:19], s6 offen offset:3072 sc1\n"
"s_waitcnt vmcnt(3)\n"
"v_bitop3_b32 v20, v48, v50, s63 bitop3:0x7e\n"
"v_bitop3_b32 v21, v49, v51, s64 bitop3:0x7e\n"
"v_and_b32 v49, s67, v49\n"
"v_and_b32 v51, s67, v51\n"
"s_waitcnt vmcnt(2)\n"
"v_bitop3_b32 v22, v52, v54, s63 bitop3:0x7e\n"
"v_bitop3_b32 v23, v53, v55, s64 bitop3:0x7e\n"
"v_and_b32 v53, s67, v53\n"
"v_and_b32 v55, s67, v55\n"
"s_waitcnt vmcnt(1)\n"
"v_bitop3_b32 v24, v56, v58, s63 bitop3:0x7e\n"
"v_bitop3_b32 v25, v57, v59, s64 bitop3:0x7e\n"
"v_and_b32 v57, s67, v57\n"
"v_and_b32 v59, s67, v59\n"
"s_waitcnt vmcnt(0)\n"
"v_bitop3_b32 v26, v60, v62, s63 bitop3:0x7e\n"
"v_bitop3_b32 v27, v61, v63, s64 bitop3:0x7e\n"
"v_and_b32 v61, s67, v61\n"
"v_and_b32 v63, s67, v63\n"
"v_or3_b32 v21, v21, v23, v25\n"
"v_permlane32_swap_b32 v49, v51\n"
"v_or3_b32 v20, v20, v22, v24\n"
"v_permlane32_swap_b32 v53, v55\n"
"v_or_b32 v21, v21, v27\n"
"v_permlane32_swap_b32 v57, v59\n"
"v_and_b32 v21, 0xff, v21\n"
"v_permlane32_swap_b32 v61, v63\n"
"v_or3_b32 v20, v20, v26, v21\n"
"v_add_f32 v49, v49, v51\n"
"v_add_f32 v53, v53, v55\n"
"v_add_f32 v57, v57, v59\n"
"v_add_f32 v61, v61, v63\n"
"v_cmp_ne_u32 vcc, 0, v20\n"
"v_cvt_pk_bf16_f32 v49, v49, v49\n"
"ds_write_b16 v5, v49 offset:0\n"
"v_cvt_pk_bf16_f32 v53, v53, v53\n"
"ds_write_b16 v5, v53 offset:128\n"
"v_cvt_pk_bf16_f32 v57, v57, v57\n"
"ds_write_b16 v5, v57 offset:256\n"
"v_cvt_pk_bf16_f32 v61, v61, v61\n"
"ds_write_b16 v5, v61 offset:384\n"
"s_cbranch_vccnz .LR_q\n"
".LD_q:\n"
"ds_write_b32 v8, v9 offset:5888\n"
"s_setprio 0\n"
"s_waitcnt vmcnt(0)\n"
"v_lshlrev_b32 v68, 9, v1\n"
"s_mov_b32 s0, 40960\n"
"s_mov_b32 s1, 73728\n"
".LP_t0:\n"
"buffer_load_dwordx2 v[64:65], v68, s[16:19], s0 offen sc1\n"
"buffer_load_dwordx2 v[66:67], v68, s[16:19], s1 offen sc1\n"
"s_waitcnt vmcnt(0)\n"
"v_bitop3_b32 v70, v64, v66, s63 bitop3:0x7e\n"
"v_bitop3_b32 v71, v65, v67, s64 bitop3:0x7e\n"
"v_and_b32 v71, 0xff, v71\n"
"v_or_b32 v70, v70, v71\n"
"v_cmp_ne_u32 vcc, 0, v70\n"
"s_cbranch_vccnz .LR_t0\n"
".LD_t0:\n"
"s_barrier\n"
"s_branch .LM2E\n"
".LAUX3:\n"
"s_cmp_gt_u32 s61, 8\n"
"s_cbranch_scc1 .LAUX3b\n"
"s_cmp_eq_u32 s3, 0\n"
"s_cbranch_scc1 .LAUX4\n"
".LAUX3b:\n"
"s_load_dwordx16 s[40:55], s[0:1], 0x80\n"
"s_load_dwordx4 s[56:59], s[0:1], 0xc0\n"
"s_cmp_lg_u32 s3, 1\n"
"s_cbranch_scc1 .LNOCL\n"
"s_lshl_b32 s4, s66, 1\n"
"s_add_u32 s4, s4, s65\n"
"s_mul_i32 s4, s4, 1152\n"
"s_add_u32 s4, s4, 131072\n"
"s_mul_i32 s5, s60, 1152\n"
"s_lshl_b32 s6, s61, 7\n"
"s_add_u32 s5, s5, s6\n"
"s_add_u32 s5, s5, 278528\n"
"s_lshl_b32 s6, s61, 3\n"
"s_add_u32 s6, s6, s60\n"
"s_lshl_b32 s7, s6, 8\n"
"s_add_u32 s7, s7, 278528\n"
"s_cmp_gt_u32 s6, 107\n"
"s_cselect_b32 s7, 0x400000, s7\n"
"s_cmp_gt_u32 s61, 8\n"
"s_cselect_b32 s5, s7, s5\n"
"v_mov_b32 v20, -1\n"
"v_mov_b32 v21, -1\n"
"v_mov_b32 v22, -1\n"
"v_mov_b32 v23, -1\n"
"buffer_store_dwordx4 v[20:23], v2, s[16:19], s4 offen sc1\n"
"s_mov_b64 exec, 1\n"
"buffer_store_dwordx4 v[20:23], v2, s[16:19], s4 offen offset:1024 sc1\n"
"s_mov_b32 s6, 0xffff\n"
"s_cselect_b32 exec_lo, s6, 0xff\n"
"buffer_store_dwordx4 v[20:23], v2, s[16:19], s5 offen sc1\n"
"s_mov_b64 exec, -1\n"
".LNOCL:\n"
"s_cmp_gt_u32 s61, 8\n"
"s_cbranch_scc0 .LNOH\n"
"s_getpc_b64 s[4:5]\n"
".LH3:\n"
"s_lshl_b32 s6, s3, 10\n"
"s_add_u32 s6, s6, .Lstart+1024-.LH3\n"
"s_ashr_i32 s7, s6, 31\n"
"s_add_u32 s4, s4, s6\n"
"s_addc_u32 s5, s5, s7\n"
"s_mov_b64 exec, 0\n"
"s_swappc_b64 vcc, s[4:5]\n"
"s_cmp_ge_u32 s3, 6\n"
"s_cbranch_scc0 .LHE3\n"
"s_add_u32 s4, s4, 0xc00\n"
"s_addc_u32 s5, s5, 0\n"
"s_swappc_b64 vcc, s[4:5]\n"
".LHE3:\n"
"s_mov_b64 exec, -1\n"
".LNOH:\n"
"v_lshrrev_b32 v19, 1, v5\n"
"v_and_b32 v7, 1, v1\n"
"v_mov_b32 v11, 0xffff\n"
"v_lshlrev_b32 v7, 4, v7\n"
"v_lshlrev_b32 v7, v7, v11\n"
"v_cmp_eq_u32 vcc, 0, v19\n"
"s_nop 1\n"
"v_cndmask_b32 v8, 0, v7, vcc\n"
"v_cmp_eq_u32 vcc, 1, v19\n"
"s_nop 1\n"
"v_cndmask_b32 v9, 0, v7, vcc\n"
"v_cmp_eq_u32 vcc, 2, v19\n"
"s_nop 1\n"
"v_cndmask_b32 v10, 0, v7, vcc\n"
"v_cmp_eq_u32 vcc, 3, v19\n"
"s_nop 1\n"
"v_cndmask_b32 v11, 0, v7, vcc\n"
"v_mov_b64 v[20:21], 0\n"
"v_lshlrev_b32 v7, 3, v1\n"
"ds_write_b64 v7, v[20:21] offset:4608\n"
"ds_write_b32 v20, v20 offset:5888\n"
"s_waitcnt vmcnt(7)\n"
"v_readfirstlane_b32 s63, v16\n"
"v_readfirstlane_b32 s64, v17\n"
"s_add_u32 s63, s63, 1\n"
"s_addc_u32 s64, s64, 0\n"
"s_and_b32 s64, s64, 0xff\n"
".LAUX4:\n"
"v_lshrrev_b32 v12, 5, v1\n"
"s_lshl_b32 s4, s3, 9\n"
"v_lshlrev_b32 v12, 8, v12\n"
"v_bfe_u32 v13, v1, 4, 1\n"
"v_lshl_or_b32 v12, v13, 7, v12\n"
"v_or_b32 v12, v12, v4\n"
"v_add_u32 v12, s4, v12\n"
"v_add_u32 v12, 0, v12\n"
"v_add_u32 v13, 0, v2\n"
"v_mov_b32 v14, s63\n"
"s_cbranch_execz .LRET\n"
"v_mov_b32 v19, s64\n"
"s_mul_i32 s4, s65, 9\n"
"s_add_u32 s4, s4, s3\n"
"s_lshl_b32 s6, s4, 11\n"
"s_add_u32 s6, s6, 4096\n"
"s_lshl_b32 s7, s66, 1\n"
"s_add_u32 s7, s7, s65\n"
"s_lshl_b32 s7, s7, 9\n"
"s_add_u32 s7, s7, 40960\n"
"v_lshlrev_b32 v6, 3, v1\n"
"s_waitcnt vmcnt(4)\n"
"s_cmp_gt_u32 s61, 8\n"
"s_cbranch_scc0 .LNOPM\n"
"buffer_load_dwordx4 v[128:131], v160, s[36:39], s8 offen nt\n"
"s_add_u32 s8, s8, s9\n"
"buffer_load_dwordx4 v[132:135], v160, s[36:39], s8 offen nt\n"
"s_add_u32 s8, s8, s9\n"
".LNOPM:\n"
"v_lshlrev_b32 v40, 16, v120\n"
"v_and_b32 v41, 0xffff0000, v120\n"
"v_lshlrev_b32 v42, 16, v121\n"
"v_and_b32 v43, 0xffff0000, v121\n"
"v_lshlrev_b32 v44, 16, v122\n"
"v_and_b32 v45, 0xffff0000, v122\n"
"v_lshlrev_b32 v46, 16, v123\n"
"v_and_b32 v47, 0xffff0000, v123\n"
"v_lshlrev_b32 v48, 16, v124\n"
"v_and_b32 v49, 0xffff0000, v124\n"
"v_lshlrev_b32 v50, 16, v125\n"
"v_and_b32 v51, 0xffff0000, v125\n"
"v_lshlrev_b32 v52, 16, v126\n"
"v_and_b32 v53, 0xffff0000, v126\n"
"v_lshlrev_b32 v54, 16, v127\n"
"v_and_b32 v55, 0xffff0000, v127\n"
"v_add_f32 v20, v40, v41\n"
"v_mul_f32 v21, v40, v40\n"
"v_fmac_f32 v21, v41, v41\n"
"v_add_f32 v20, v20, v42\n"
"v_fmac_f32 v21, v42, v42\n"
"v_add_f32 v20, v20, v43\n"
"v_fmac_f32 v21, v43, v43\n"
"v_add_f32 v20, v20, v44\n"
"v_fmac_f32 v21, v44, v44\n"
"v_add_f32 v20, v20, v45\n"
"v_fmac_f32 v21, v45, v45\n"
"v_add_f32 v20, v20, v46\n"
"v_fmac_f32 v21, v46, v46\n"
"v_add_f32 v20, v20, v47\n"
"v_fmac_f32 v21, v47, v47\n"
"v_add_f32 v20, v20, v48\n"
"v_fmac_f32 v21, v48, v48\n"
"v_add_f32 v20, v20, v49\n"
"v_fmac_f32 v21, v49, v49\n"
"v_add_f32 v20, v20, v50\n"
"v_fmac_f32 v21, v50, v50\n"
"v_add_f32 v20, v20, v51\n"
"v_fmac_f32 v21, v51, v51\n"
"v_add_f32 v20, v20, v52\n"
"v_fmac_f32 v21, v52, v52\n"
"v_add_f32 v20, v20, v53\n"
"v_fmac_f32 v21, v53, v53\n"
"v_add_f32 v20, v20, v54\n"
"v_fmac_f32 v21, v54, v54\n"
"v_add_f32 v20, v20, v55\n"
"v_fmac_f32 v21, v55, v55\n"
"s_nop 1\n"
"v_permlane32_swap_b32 v20, v21\n"
"v_add_f32 v20, v20, v21\n"
"v_mov_b32 v21, v20\n"
"s_nop 1\n"
"v_permlane16_swap_b32 v20, v21\n"
"v_add_f32 v20, v20, v21\n"
"s_nop 1\n"
"v_add_f32_dpp v20, v20, v20 quad_perm:[1,0,3,2] row_mask:0xf bank_mask:0xf\n"
"s_nop 1\n"
"v_add_f32_dpp v20, v20, v20 quad_perm:[2,3,0,1] row_mask:0xf bank_mask:0xf\n"
"s_nop 1\n"
"v_add_f32_dpp v20, v20, v20 row_half_mirror row_mask:0xf bank_mask:0xf\n"
"s_nop 1\n"
"v_add_f32_dpp v20, v20, v20 row_ror:8 row_mask:0xf bank_mask:0xf\n"
"v_mul_f32 v20, 0x3ae38e39, v20\n"
"v_mov_b32 v21, v20\n"
"s_nop 1\n"
"v_permlane32_swap_b32 v20, v21\n"
"v_fma_f32 v21, -v20, v20, v21\n"
"v_max_f32 v21, 0, v21\n"
"v_cvt_pk_bf16_f32 v21, v21, v21\n"
"v_lshlrev_b32 v21, 16, v21\n"
"v_add_f32 v21, 0x3727c5ac, v21\n"
"v_cvt_pk_bf16_f32 v21, v21, v21\n"
"v_lshlrev_b32 v21, 16, v21\n"
"v_sqrt_f32 v21, v21\n"
"s_nop 0\n"
"v_cvt_pk_bf16_f32 v21, v21, v21\n"
"v_lshlrev_b32 v21, 16, v21\n"
"v_rcp_f32 v21, v21\n"
"v_cvt_pk_bf16_f32 v20, v20, v20\n"
"v_lshlrev_b32 v20, 16, v20\n"
"v_sub_f32 v18, v18, v20\n"
"v_cvt_pk_bf16_f32 v18, v18, v18\n"
"v_lshlrev_b32 v18, 16, v18\n"
"v_mul_f32 v18, v18, v21\n"
"v_cvt_pk_bf16_f32 v18, v18, v18\n"
"v_lshlrev_b32 v18, 16, v18\n"
"s_waitcnt vmcnt(0)\n"
".LP_h:\n"
"buffer_load_dwordx4 v[32:35], v2, s[16:19], s6 offen sc1\n"
"buffer_load_dwordx4 v[36:39], v2, s[16:19], s6 offen offset:1024 sc1\n"
"s_waitcnt vmcnt(0)\n"
"v_bitop3_b32 v20, v32, v34, s63 bitop3:0x7e\n"
"v_bitop3_b32 v21, v33, v35, s64 bitop3:0x7e\n"
"v_bitop3_b32 v22, v36, v38, s63 bitop3:0x7e\n"
"v_bitop3_b32 v23, v37, v39, s64 bitop3:0x7e\n"
"v_or_b32 v20, v20, v22\n"
"v_or_b32 v21, v21, v23\n"
"v_and_b32 v21, 0xff, v21\n"
"v_or_b32 v20, v20, v21\n"
"v_cmp_ne_u32 vcc, 0, v20\n"
"v_and_b32 v33, s67, v33\n"
"v_and_b32 v35, s67, v35\n"
"v_and_b32 v37, s67, v37\n"
"v_and_b32 v39, s67, v39\n"
"s_cbranch_vccnz .LR_h\n"
".LD_h:\n"
"v_permlane32_swap_b32 v33, v37\n"
"v_permlane32_swap_b32 v35, v39\n"
"v_add_f32 v33, v33, v37\n"
"v_add_f32 v35, v35, v39\n"
"s_nop 1\n"
"v_permlane16_swap_b32 v33, v35\n"
"v_add_f32 v33, v33, v35\n"
"v_cvt_pk_bf16_f32 v33, v33, v33\n"
"v_lshlrev_b32 v33, 16, v33\n"
"v_mov_b32 v34, v33\n"
"s_nop 1\n"
"v_permlane32_swap_b32 v33, v34\n"
"v_add_f32 v34, 1.0, v34\n"
"v_cvt_pk_bf16_f32 v34, v34, v34\n"
"v_lshlrev_b32 v34, 16, v34\n"
"v_mul_f32 v34, v18, v34\n"
"v_cvt_pk_bf16_f32 v34, v34, v34\n"
"v_lshlrev_b32 v34, 16, v34\n"
"v_add_f32 v33, v33, v34\n"
"v_cvt_pk_bf16_f32 v33, v33, v33\n"
"s_nop 1\n"
"v_mov_b32_dpp v33, v33 row_shr:4 row_mask:0x3 bank_mask:0xa\n"
"s_nop 1\n"
"v_mov_b32_dpp v33, v33 row_shl:4 row_mask:0xc bank_mask:0x5\n"
"s_nop 1\n"
"v_and_b32_dpp v20, v33, v8 quad_perm:[0,0,0,0] row_mask:0xf bank_mask:0xf\n"
"v_and_b32_dpp v21, v33, v9 quad_perm:[0,0,0,0] row_mask:0xf bank_mask:0xf\n"
"v_and_b32_dpp v22, v33, v10 quad_perm:[0,0,0,0] row_mask:0xf bank_mask:0xf\n"
"v_and_b32_dpp v23, v33, v11 quad_perm:[0,0,0,0] row_mask:0xf bank_mask:0xf\n"
"v_and_b32_dpp v24, v33, v8 quad_perm:[1,1,1,1] row_mask:0xf bank_mask:0xf\n"
"v_and_b32_dpp v25, v33, v9 quad_perm:[1,1,1,1] row_mask:0xf bank_mask:0xf\n"
"v_and_b32_dpp v26, v33, v10 quad_perm:[1,1,1,1] row_mask:0xf bank_mask:0xf\n"
"v_and_b32_dpp v27, v33, v11 quad_perm:[1,1,1,1] row_mask:0xf bank_mask:0xf\n"
"v_mfma_f32_16x16x32_bf16 v[28:31], v[104:107], v[20:23], 0\n"
"v_and_b32_dpp v20, v33, v8 quad_perm:[2,2,2,2] row_mask:0xf bank_mask:0xf\n"
"v_and_b32_dpp v21, v33, v9 quad_perm:[2,2,2,2] row_mask:0xf bank_mask:0xf\n"
"v_and_b32_dpp v22, v33, v10 quad_perm:[2,2,2,2] row_mask:0xf bank_mask:0xf\n"
"v_and_b32_dpp v23, v33, v11 quad_perm:[2,2,2,2] row_mask:0xf bank_mask:0xf\n"
"v_mfma_f32_16x16x32_bf16 v[28:31], v[108:111], v[24:27], v[28:31]\n"
"v_and_b32_dpp v24, v33, v8 quad_perm:[3,3,3,3] row_mask:0xf bank_mask:0xf\n"
"v_and_b32_dpp v25, v33, v9 quad_perm:[3,3,3,3] row_mask:0xf bank_mask:0xf\n"
"v_and_b32_dpp v26, v33, v10 quad_perm:[3,3,3,3] row_mask:0xf bank_mask:0xf\n"
"v_and_b32_dpp v27, v33, v11 quad_perm:[3,3,3,3] row_mask:0xf bank_mask:0xf\n"
"v_mfma_f32_16x16x32_bf16 v[28:31], v[112:115], v[20:23], v[28:31]\n"
"s_nop 0\n"
"v_mfma_f32_16x16x32_bf16 v[28:31], v[116:119], v[24:27], v[28:31]\n"
"s_mov_b32 exec_lo, 0xff00ff\n"
"s_mov_b32 exec_hi, 0xff00ff00\n"
"s_nop 5\n"
"ds_write_b128 v12, v[28:31]\n"
"s_mov_b64 exec, -1\n"
"s_waitcnt lgkmcnt(0)\n"
"s_barrier\n"
"s_cmp_lg_u32 s3, 0\n"
"s_cbranch_scc1 .LAUX5\n"
"ds_read_b128 v[32:35], v13 offset:0\n"
"ds_read_b128 v[36:39], v13 offset:1024\n"
"ds_read_b128 v[40:43], v13 offset:2048\n"
"ds_read_b128 v[44:47], v13 offset:3072\n"
"ds_read_b128 v[48:51], v13 offset:4096\n"
"s_waitcnt lgkmcnt(3)\n"
"v_add_f32 v32, v32, v36\n"
"v_add_f32 v33, v33, v37\n"
"v_add_f32 v34, v34, v38\n"
"v_add_f32 v35, v35, v39\n"
"s_waitcnt lgkmcnt(1)\n"
"v_add_f32 v40, v40, v44\n"
"v_add_f32 v41, v41, v45\n"
"s_cbranch_execz .LRET\n"
"v_add_f32 v42, v42, v46\n"
"v_add_f32 v43, v43, v47\n"
"s_waitcnt lgkmcnt(0)\n"
"v_add_f32 v32, v32, v48\n"
"v_add_f32 v33, v33, v49\n"
"v_add_f32 v34, v34, v50\n"
"v_add_f32 v35, v35, v51\n"
"v_add_f32 v32, v32, v40\n"
"v_add_f32 v33, v33, v41\n"
"v_add_f32 v34, v34, v42\n"
"v_add_f32 v35, v35, v43\n"
"v_permlane32_swap_b32 v32, v33\n"
"s_nop 0\n"
"v_permlane32_swap_b32 v34, v35\n"
"v_add_f32 v32, v32, v33\n"
"v_add_f32 v34, v34, v35\n"
"s_nop 1\n"
"v_permlane16_swap_b32 v32, v34\n"
"v_add_f32 v20, v32, v34\n"
"v_bfi_b32 v15, s67, v20, v19\n"
"buffer_store_dwordx2 v[14:15], v6, s[16:19], s7 offen sc1\n"
".LAUX5:\n"
"s_barrier\n"
"s_cmp_gt_u32 s61, 8\n"
"s_cbranch_scc1 .LDL1\n"
"s_sleep 26\n"
"s_branch .LDL2\n"
".LDL1:\n"
".LDL2:\n"
"s_cmp_gt_u32 s61, 8\n"
"s_cbranch_scc1 .LNOPN\n"
"buffer_load_dwordx4 v[128:131], v160, s[36:39], s8 offen nt\n"
"s_add_u32 s8, s8, s9\n"
"buffer_load_dwordx4 v[132:135], v160, s[36:39], s8 offen nt\n"
"s_add_u32 s8, s8, s9\n"
".LNOPN:\n"
"buffer_load_dwordx4 v[136:139], v160, s[36:39], s8 offen nt\n"
"s_add_u32 s8, s8, s9\n"
"buffer_load_dwordx4 v[140:143], v160, s[36:39], s8 offen nt\n"
"s_add_u32 s8, s8, s9\n"
"buffer_load_dwordx4 v[144:147], v160, s[36:39], s8 offen nt\n"
"s_add_u32 s8, s8, s9\n"
"buffer_load_dwordx4 v[148:151], v160, s[36:39], s8 offen nt\n"
"s_add_u32 s8, s8, s9\n"
"buffer_load_dwordx4 v[152:155], v160, s[36:39], s8 offen nt\n"
"s_add_u32 s8, s8, s9\n"
"buffer_load_dwordx4 v[156:159], v160, s[36:39], s8 offen nt\n"
"s_add_u32 s8, s8, s9\n"
"s_lshl_b32 s4, s61, 3\n"
"s_add_u32 s4, s4, s60\n"
"s_add_u32 s65, s4, 56\n"
"s_sub_u32 s66, s4, 72\n"
"s_cmp_gt_u32 s61, 8\n"
"s_cbranch_scc1 .LM2\n"
"s_cmp_eq_u32 s61, 8\n"
"s_cselect_b32 s65, 184, -1\n"
"s_cselect_b32 s4, s60, 0\n"
"s_add_u32 s65, s65, s4\n"
"s_cmp_eq_u32 s3, 8\n"
"s_cbranch_scc1 .LS4W8\n"
"s_mul_i32 s5, s3, 9216\n"
"s_add_u32 s5, s5, 20112\n"
"v_mul_u32_u24 v6, 144, v3\n"
"v_add3_u32 v6, v6, v4, s5\n"
"v_lshrrev_b32 v7, 5, v1\n"
"v_lshlrev_b32 v7, 5, v7\n"
"v_bfe_u32 v8, v1, 4, 1\n"
"v_or_b32 v7, v7, v8\n"
"v_bfe_u32 v8, v1, 2, 2\n"
"v_lshrrev_b32 v9, 1, v8\n"
"v_and_b32 v8, 1, v8\n"
"v_lshl_or_b32 v7, v9, 2, v7\n"
"v_lshl_or_b32 v7, v8, 1, v7\n"
"v_mul_u32_u24 v7, 144, v7\n"
"v_and_b32 v8, 3, v1\n"
"v_lshl_add_u32 v7, v8, 3, v7\n"
"v_add_u32 v7, s5, v7\n"
"s_waitcnt vmcnt(7)\n"
"ds_write_b128 v6, v[128:131] offset:0\n"
"s_waitcnt vmcnt(6)\n"
"ds_write_b128 v6, v[132:135] offset:1152\n"
"s_waitcnt vmcnt(5)\n"
"ds_write_b128 v6, v[136:139] offset:2304\n"
"s_waitcnt vmcnt(4)\n"
"ds_write_b128 v6, v[140:143] offset:3456\n"
"s_waitcnt vmcnt(3)\n"
"ds_write_b128 v6, v[144:147] offset:4608\n"
"s_waitcnt vmcnt(2)\n"
"ds_write_b128 v6, v[148:151] offset:5760\n"
"s_waitcnt vmcnt(1)\n"
"ds_write_b128 v6, v[152:155] offset:6912\n"
"s_waitcnt vmcnt(0)\n"
"ds_write_b128 v6, v[156:159] offset:8064\n"
"ds_read_b64_tr_b16 v[64:65], v7 offset:0\n"
"ds_read_b64_tr_b16 v[66:67], v7 offset:1152\n"
"ds_read_b64_tr_b16 v[68:69], v7 offset:32\n"
"ds_read_b64_tr_b16 v[70:71], v7 offset:1184\n"
"ds_read_b64_tr_b16 v[72:73], v7 offset:64\n"
"ds_read_b64_tr_b16 v[74:75], v7 offset:1216\n"
"ds_read_b64_tr_b16 v[76:77], v7 offset:96\n"
"ds_read_b64_tr_b16 v[78:79], v7 offset:1248\n"
"ds_read_b64_tr_b16 v[80:81], v7 offset:2304\n"
"ds_read_b64_tr_b16 v[82:83], v7 offset:3456\n"
"ds_read_b64_tr_b16 v[84:85], v7 offset:2336\n"
"ds_read_b64_tr_b16 v[86:87], v7 offset:3488\n"
"ds_read_b64_tr_b16 v[88:89], v7 offset:2368\n"
"ds_read_b64_tr_b16 v[90:91], v7 offset:3520\n"
"ds_read_b64_tr_b16 v[92:93], v7 offset:2400\n"
"ds_read_b64_tr_b16 v[94:95], v7 offset:3552\n"
"s_cmp_lt_i32 s65, 0\n"
"s_cbranch_scc1 .LS4P\n"
"s_getpc_b64 s[0:1]\n"
".LS4Pq:\n"
"s_add_u32 s0, s0, .LS4P-.LS4Pq\n"
"s_addc_u32 s1, s1, 0\n"
"s_branch .LPAR\n"
".LS4P:\n"
"s_lshr_b32 s4, s3, 2\n"
"s_lshl_b32 s5, s60, 1\n"
"s_add_u32 s4, s4, s5\n"
"s_mul_i32 s6, s4, 9216\n"
"s_add_u32 s6, s6, 131072\n"
"s_and_b32 s4, s3, 3\n"
"s_lshl_b32 s4, s4, 8\n"
"v_mul_u32_u24 v8, 1152, v3\n"
"v_add3_u32 v9, v8, v4, s4\n"
"s_and_b32 s4, s3, 2\n"
"s_lshl_b32 s4, s4, 2\n"
"v_add_u32 v8, s4, v8\n"
"s_lshl_b32 s4, s3, 8\n"
"s_add_u32 s4, s4, 18064\n"
"v_add_u32 v10, s4, v2\n"
"v_add_u32 v11, 18064, v2\n"
"s_mul_i32 s7, s60, 1152\n"
"s_lshl_b32 s4, s61, 7\n"
"s_add_u32 s7, s7, s4\n"
"s_add_u32 s7, s7, 278528\n"
"v_lshlrev_b32 v12, 1, v1\n"
"v_mov_b32 v13, 0\n"
"v_mov_b32 v14, 1\n"
"s_waitcnt lgkmcnt(0)\n"
"s_barrier\n"
".LP_o:\n"
"buffer_load_dwordx2 v[40:41], v8, s[16:19], s6 offen offset:1024 sc1\n"
"buffer_load_dwordx4 v[32:35], v9, s[16:19], s6 offen sc1\n"
"buffer_load_dwordx4 v[36:39], v9, s[16:19], s6 offen offset:128 sc1\n"
".LQ_o:\n"
"s_waitcnt vmcnt(2)\n"
"v_max_f32_dpp v20, v40, v40 row_ror:8 row_mask:0xf bank_mask:0xf\n"
"v_max_u32 v24, v40, v41\n"
"v_mov_b32 v21, v20\n"
"s_waitcnt vmcnt(1)\n"
"v_max3_u32 v24, v24, v32, v33\n"
"v_permlane16_swap_b32 v20, v21\n"
"v_max3_u32 v24, v24, v34, v35\n"
"v_max_f32 v20, v20, v21\n"
"v_mov_b32 v21, v20\n"
"s_waitcnt vmcnt(0)\n"
"v_max3_u32 v24, v24, v36, v37\n"
"v_permlane32_swap_b32 v20, v21\n"
"v_max3_u32 v24, v24, v38, v39\n"
"v_max_f32 v20, v20, v21\n"
"v_cmp_eq_u32 vcc, -1, v24\n"
"v_sub_f32 v20, v40, v20\n"
"v_exp_f32 v20, v20\n"
"s_cbranch_vccnz .LR_o\n"
".LD_o:\n"
"v_mul_f32 v22, v20, v41\n"
"v_mul_f32 v32, v20, v32\n"
"v_mul_f32 v33, v20, v33\n"
"v_mul_f32 v34, v20, v34\n"
"v_mul_f32 v35, v20, v35\n"
"v_mul_f32 v36, v20, v36\n"
"v_mul_f32 v37, v20, v37\n"
"v_mul_f32 v38, v20, v38\n"
"v_mul_f32 v39, v20, v39\n"
"v_add_f32_dpp v22, v22, v22 row_ror:8 row_mask:0xf bank_mask:0xf\n"
"v_permlane32_swap_b32 v32, v36\n"
"v_permlane32_swap_b32 v33, v37\n"
"v_permlane32_swap_b32 v34, v38\n"
"v_permlane32_swap_b32 v35, v39\n"
"v_mov_b32 v23, v22\n"
"v_add_f32 v32, v32, v36\n"
"v_add_f32 v33, v33, v37\n"
"v_add_f32 v34, v34, v38\n"
"v_add_f32 v35, v35, v39\n"
"v_permlane16_swap_b32 v22, v23\n"
"v_permlane16_swap_b32 v32, v33\n"
"v_permlane16_swap_b32 v34, v35\n"
"v_add_f32 v22, v22, v23\n"
"v_add_f32 v32, v32, v33\n"
"v_mov_b32 v23, v22\n"
"v_add_f32 v34, v34, v35\n"
"v_add_f32_dpp v32, v32, v32 row_ror:8 row_mask:0xf bank_mask:0xf\n"
"v_permlane32_swap_b32 v22, v23\n"
"v_add_f32_dpp v34, v34, v34 row_ror:8 row_mask:0xf bank_mask:0xf\n"
"v_add_f32 v22, v22, v23\n"
"v_rcp_f32 v22, v22\n"
"s_cbranch_execz .LRET\n"
"s_nop 0\n"
"v_mul_f32 v32, v32, v22\n"
"v_mul_f32 v34, v34, v22\n"
"v_cvt_pk_bf16_f32 v24, v32, v34\n"
"s_nop 1\n"
"v_mov_b32_dpp v25, v24 row_shl:1 row_mask:0xf bank_mask:0xf bound_ctrl:0\n"
"v_mov_b32_dpp v26, v24 row_shl:2 row_mask:0xf bank_mask:0xf bound_ctrl:0\n"
"v_mov_b32_dpp v27, v24 row_shl:3 row_mask:0xf bank_mask:0xf bound_ctrl:0\n"
"v_mov_b32_dpp v28, v24 row_shl:4 row_mask:0xf bank_mask:0xf bound_ctrl:0\n"
"v_mov_b32_dpp v29, v24 row_shl:5 row_mask:0xf bank_mask:0xf bound_ctrl:0\n"
"v_mov_b32_dpp v30, v24 row_shl:6 row_mask:0xf bank_mask:0xf bound_ctrl:0\n"
"v_mov_b32_dpp v31, v24 row_shl:7 row_mask:0xf bank_mask:0xf bound_ctrl:0\n"
"v_mfma_f32_16x16x32_bf16 v[32:35], v[24:27], v[64:67], 0\n"
"v_mfma_f32_16x16x32_bf16 v[36:39], v[24:27], v[68:71], 0\n"
"v_mfma_f32_16x16x32_bf16 v[40:43], v[24:27], v[72:75], 0\n"
"v_mfma_f32_16x16x32_bf16 v[44:47], v[24:27], v[76:79], 0\n"
"v_mfma_f32_16x16x32_bf16 v[32:35], v[28:31], v[80:83], v[32:35]\n"
"v_mfma_f32_16x16x32_bf16 v[36:39], v[28:31], v[84:87], v[36:39]\n"
"v_mfma_f32_16x16x32_bf16 v[40:43], v[28:31], v[88:91], v[40:43]\n"
"v_mfma_f32_16x16x32_bf16 v[44:47], v[28:31], v[92:95], v[44:47]\n"
"s_mov_b64 exec, 0xffff\n"
"s_nop 5\n"
"ds_write2_b32 v10, v32, v40 offset1:1\n"
"ds_write2_b32 v10, v36, v44 offset0:2 offset1:3\n"
"s_mov_b64 exec, 1\n"
"ds_add_u32 v13, v14 offset:5888\n"
"s_mov_b64 exec, -1\n"
"s_cmp_lg_u32 s3, 0\n"
"s_cbranch_scc1 .LS4E\n"
".LP_c:\n"
"ds_read_b32 v40, v13 offset:5888\n"
"ds_read_b128 v[32:35], v11\n"
"ds_read_b128 v[36:39], v11 offset:1024\n"
"s_waitcnt lgkmcnt(2)\n"
"v_readfirstlane_b32 s4, v40\n"
"s_cmp_lg_u32 s4, 8\n"
"s_cbranch_scc1 .LP_c\n"
"s_waitcnt lgkmcnt(0)\n"
"v_add_f32 v32, v32, v36\n"
"v_add_f32 v33, v33, v37\n"
"v_add_f32 v34, v34, v38\n"
"v_add_f32 v35, v35, v39\n"
"v_permlane32_swap_b32 v32, v33\n"
"s_nop 0\n"
"v_permlane32_swap_b32 v34, v35\n"
"v_add_f32 v32, v32, v33\n"
"v_add_f32 v34, v34, v35\n"
"s_nop 1\n"
"v_permlane16_swap_b32 v32, v34\n"
"v_add_f32 v32, v32, v34\n"
"s_nop 1\n"
"v_mov_b32_dpp v33, v32 quad_perm:[1,0,3,2] row_mask:0xf bank_mask:0xf\n"
"v_cvt_pk_bf16_f32 v24, v32, v33\n"
"v_min_u32 v24, -2, v24\n"
"s_nop 1\n"
"v_mov_b32_dpp v25, v24 row_shl:2 row_mask:0xf bank_mask:0xf bound_ctrl:0\n"
"v_mov_b32_dpp v26, v24 row_shl:4 row_mask:0xf bank_mask:0xf bound_ctrl:0\n"
"v_mov_b32_dpp v27, v24 row_shl:6 row_mask:0xf bank_mask:0xf bound_ctrl:0\n"
"s_mov_b32 exec_lo, 0x1010101\n"
"s_mov_b32 exec_hi, 0x1010101\n"
"buffer_store_dwordx4 v[24:27], v12, s[16:19], s7 offen sc1\n"
"s_mov_b64 exec, -1\n"
".LS4E:\n"
"s_cmp_lt_i32 s65, 0\n"
"s_cbranch_scc1 .LEND\n"
"s_branch .LS5B\n"
".LS4W8:\n"
"v_lshlrev_b32 v68, 9, v1\n"
"s_mov_b32 s0, 40960\n"
"s_mov_b32 s1, 73728\n"
".LP_t1:\n"
"buffer_load_dwordx2 v[64:65], v68, s[16:19], s0 offen sc1\n"
"buffer_load_dwordx2 v[66:67], v68, s[16:19], s1 offen sc1\n"
"s_waitcnt vmcnt(0)\n"
"v_bitop3_b32 v70, v64, v66, s63 bitop3:0x7e\n"
"v_bitop3_b32 v71, v65, v67, s64 bitop3:0x7e\n"
"v_and_b32 v71, 0xff, v71\n"
"v_or_b32 v70, v70, v71\n"
"v_cmp_ne_u32 vcc, 0, v70\n"
"s_cbranch_vccnz .LR_t1\n"
".LD_t1:\n"
"s_barrier\n"
"s_cmp_lt_i32 s65, 0\n"
"s_cbranch_scc1 .LEND\n"
"s_branch .LS5M\n"
".LM2:\n"
"v_lshl_add_u32 v7, v5, 3, v3\n"
"v_mov_b32 v16, 0\n"
"v_lshlrev_b32 v7, 1, v7\n"
"s_lshl_b32 s6, s3, 7\n"
"buffer_load_short_d16_hi v16, v7, s[28:31], s6 offen\n"
".LM2N:\n"
"s_cmp_lg_u32 s3, 8\n"
"s_cbranch_scc1 .LM2B\n"
"v_lshlrev_b32 v68, 9, v1\n"
"s_mov_b32 s0, 40960\n"
"s_mov_b32 s1, 73728\n"
".LP_t2:\n"
"buffer_load_dwordx2 v[64:65], v68, s[16:19], s0 offen sc1\n"
"buffer_load_dwordx2 v[66:67], v68, s[16:19], s1 offen sc1\n"
"s_waitcnt vmcnt(0)\n"
"v_bitop3_b32 v70, v64, v66, s63 bitop3:0x7e\n"
"v_bitop3_b32 v71, v65, v67, s64 bitop3:0x7e\n"
"v_and_b32 v71, 0xff, v71\n"
"v_or_b32 v70, v70, v71\n"
"v_cmp_ne_u32 vcc, 0, v70\n"
"s_cbranch_vccnz .LR_t2\n"
".LD_t2:\n"
".LM2B:\n"
"s_cmp_gt_u32 s66, 35\n"
"s_cbranch_scc1 .LS5A\n"
"s_waitcnt vmcnt(0)\n"
"v_mul_f32 v17, 0xbfb8aa3b, v16\n"
"v_exp_f32 v17, v17\n"
"s_nop 0\n"
"v_add_f32 v17, 1.0, v17\n"
"v_rcp_f32 v17, v17\n"
"s_nop 0\n"
"v_mul_f32 v17, v16, v17\n"
"v_cvt_pk_bf16_f32 v16, v17, v17\n"
"v_mov_b32 v17, v16\n"
"s_nop 0\n"
"v_mov_b32_dpp v16, v16 row_shr:4 row_mask:0xf bank_mask:0xa\n"
"v_mov_b32_dpp v17, v17 row_shl:4 row_mask:0xf bank_mask:0x5\n"
"s_nop 0\n"
"v_and_b32_dpp v20, v16, v8 quad_perm:[0,0,0,0] row_mask:0xf bank_mask:0xf\n"
"v_and_b32_dpp v21, v16, v9 quad_perm:[0,0,0,0] row_mask:0xf bank_mask:0xf\n"
"v_and_b32_dpp v22, v16, v10 quad_perm:[0,0,0,0] row_mask:0xf bank_mask:0xf\n"
"v_and_b32_dpp v23, v16, v11 quad_perm:[0,0,0,0] row_mask:0xf bank_mask:0xf\n"
"v_and_b32_dpp v24, v16, v8 quad_perm:[1,1,1,1] row_mask:0xf bank_mask:0xf\n"
"v_and_b32_dpp v25, v16, v9 quad_perm:[1,1,1,1] row_mask:0xf bank_mask:0xf\n"
"v_and_b32_dpp v26, v16, v10 quad_perm:[1,1,1,1] row_mask:0xf bank_mask:0xf\n"
"v_and_b32_dpp v27, v16, v11 quad_perm:[1,1,1,1] row_mask:0xf bank_mask:0xf\n"
"v_mfma_f32_16x16x32_bf16 v[28:31], v[128:131], v[20:23], 0\n"
"v_and_b32_dpp v20, v16, v8 quad_perm:[2,2,2,2] row_mask:0xf bank_mask:0xf\n"
"v_and_b32_dpp v21, v16, v9 quad_perm:[2,2,2,2] row_mask:0xf bank_mask:0xf\n"
"v_and_b32_dpp v22, v16, v10 quad_perm:[2,2,2,2] row_mask:0xf bank_mask:0xf\n"
"v_and_b32_dpp v23, v16, v11 quad_perm:[2,2,2,2] row_mask:0xf bank_mask:0xf\n"
"v_mfma_f32_16x16x32_bf16 v[28:31], v[132:135], v[24:27], v[28:31]\n"
"v_and_b32_dpp v24, v16, v8 quad_perm:[3,3,3,3] row_mask:0xf bank_mask:0xf\n"
"v_and_b32_dpp v25, v16, v9 quad_perm:[3,3,3,3] row_mask:0xf bank_mask:0xf\n"
"v_and_b32_dpp v26, v16, v10 quad_perm:[3,3,3,3] row_mask:0xf bank_mask:0xf\n"
"v_and_b32_dpp v27, v16, v11 quad_perm:[3,3,3,3] row_mask:0xf bank_mask:0xf\n"
"v_mfma_f32_16x16x32_bf16 v[28:31], v[136:139], v[20:23], v[28:31]\n"
"v_and_b32_dpp v20, v17, v8 quad_perm:[0,0,0,0] row_mask:0xf bank_mask:0xf\n"
"v_and_b32_dpp v21, v17, v9 quad_perm:[0,0,0,0] row_mask:0xf bank_mask:0xf\n"
"v_and_b32_dpp v22, v17, v10 quad_perm:[0,0,0,0] row_mask:0xf bank_mask:0xf\n"
"v_and_b32_dpp v23, v17, v11 quad_perm:[0,0,0,0] row_mask:0xf bank_mask:0xf\n"
"v_mfma_f32_16x16x32_bf16 v[28:31], v[140:143], v[24:27], v[28:31]\n"
"v_and_b32_dpp v24, v17, v8 quad_perm:[1,1,1,1] row_mask:0xf bank_mask:0xf\n"
"v_and_b32_dpp v25, v17, v9 quad_perm:[1,1,1,1] row_mask:0xf bank_mask:0xf\n"
"v_and_b32_dpp v26, v17, v10 quad_perm:[1,1,1,1] row_mask:0xf bank_mask:0xf\n"
"v_and_b32_dpp v27, v17, v11 quad_perm:[1,1,1,1] row_mask:0xf bank_mask:0xf\n"
"v_mfma_f32_16x16x32_bf16 v[28:31], v[144:147], v[20:23], v[28:31]\n"
"v_and_b32_dpp v20, v17, v8 quad_perm:[2,2,2,2] row_mask:0xf bank_mask:0xf\n"
"v_and_b32_dpp v21, v17, v9 quad_perm:[2,2,2,2] row_mask:0xf bank_mask:0xf\n"
"v_and_b32_dpp v22, v17, v10 quad_perm:[2,2,2,2] row_mask:0xf bank_mask:0xf\n"
"v_and_b32_dpp v23, v17, v11 quad_perm:[2,2,2,2] row_mask:0xf bank_mask:0xf\n"
"v_mfma_f32_16x16x32_bf16 v[28:31], v[148:151], v[24:27], v[28:31]\n"
"v_and_b32_dpp v24, v17, v8 quad_perm:[3,3,3,3] row_mask:0xf bank_mask:0xf\n"
"v_and_b32_dpp v25, v17, v9 quad_perm:[3,3,3,3] row_mask:0xf bank_mask:0xf\n"
"v_and_b32_dpp v26, v17, v10 quad_perm:[3,3,3,3] row_mask:0xf bank_mask:0xf\n"
"v_and_b32_dpp v27, v17, v11 quad_perm:[3,3,3,3] row_mask:0xf bank_mask:0xf\n"
"v_mfma_f32_16x16x32_bf16 v[28:31], v[152:155], v[20:23], v[28:31]\n"
"s_nop 0\n"
"v_mfma_f32_16x16x32_bf16 v[28:31], v[156:159], v[24:27], v[28:31]\n"
"s_mov_b32 exec_lo, 0xff00ff\n"
"s_mov_b32 exec_hi, 0xff00ff00\n"
"s_nop 5\n"
"ds_write_b128 v12, v[28:31]\n"
"s_mov_b64 exec, -1\n"
"s_cbranch_execz .LRET\n"
"s_waitcnt lgkmcnt(0)\n"
"s_barrier\n"
"s_cmp_lg_u32 s3, 0\n"
"s_cbranch_scc1 .LM2E\n"
"ds_read_b128 v[32:35], v13 offset:0\n"
"ds_read_b128 v[36:39], v13 offset:1024\n"
"ds_read_b128 v[40:43], v13 offset:2048\n"
"ds_read_b128 v[44:47], v13 offset:3072\n"
"ds_read_b128 v[48:51], v13 offset:4096\n"
"s_waitcnt lgkmcnt(3)\n"
"v_add_f32 v32, v32, v36\n"
"v_add_f32 v33, v33, v37\n"
"v_add_f32 v34, v34, v38\n"
"v_add_f32 v35, v35, v39\n"
"s_waitcnt lgkmcnt(1)\n"
"v_add_f32 v40, v40, v44\n"
"v_add_f32 v41, v41, v45\n"
"v_add_f32 v42, v42, v46\n"
"v_add_f32 v43, v43, v47\n"
"s_waitcnt lgkmcnt(0)\n"
"v_add_f32 v32, v32, v48\n"
"v_add_f32 v33, v33, v49\n"
"v_add_f32 v34, v34, v50\n"
"v_add_f32 v35, v35, v51\n"
"v_add_f32 v32, v32, v40\n"
"v_add_f32 v33, v33, v41\n"
"v_add_f32 v34, v34, v42\n"
"v_add_f32 v35, v35, v43\n"
"v_permlane32_swap_b32 v32, v33\n"
"s_nop 0\n"
"v_permlane32_swap_b32 v34, v35\n"
"v_add_f32 v32, v32, v33\n"
"v_add_f32 v34, v34, v35\n"
"s_nop 1\n"
"v_permlane16_swap_b32 v32, v34\n"
"v_add_f32 v20, v32, v34\n"
"v_cvt_pk_bf16_f32 v20, v20, v20\n"
"v_lshlrev_b32 v20, 16, v20\n"
"s_lshl_b32 s4, s66, 8\n"
"s_add_u32 s4, s4, 296960\n"
"v_lshlrev_b32 v6, 2, v1\n"
"v_min_u32 v24, -2, v20\n"
"s_nop 1\n"
"v_mov_b32_dpp v25, v24 quad_perm:[1,1,1,1] row_mask:0xf bank_mask:0xf\n"
"v_mov_b32_dpp v26, v24 quad_perm:[2,2,2,2] row_mask:0xf bank_mask:0xf\n"
"v_mov_b32_dpp v27, v24 quad_perm:[3,3,3,3] row_mask:0xf bank_mask:0xf\n"
"s_mov_b32 exec_lo, 0x11111111\n"
"s_mov_b32 exec_hi, 0x11111111\n"
"buffer_store_dwordx4 v[24:27], v6, s[16:19], s4 offen sc1\n"
"s_mov_b64 exec, -1\n"
".LM2E:\n"
"s_barrier\n"
"s_branch .LS5A\n"
".LPAR:\n"
"s_waitcnt lgkmcnt(0)\n"
"v_lshlrev_b32 v96, 4, v0\n"
"v_lshlrev_b32 v97, 1, v0\n"
"v_mov_b32 v134, 0\n"
"v_mov_b32 v144, 0\n"
"v_mov_b32 v154, 0\n"
"s_movk_i32 s4, 0x2000\n"
"buffer_load_short_d16_hi v134, v97, s[40:43], s4 offen\n"
"buffer_load_short_d16_hi v144, v97, s[44:47], s4 offen\n"
"buffer_load_short_d16_hi v154, v97, s[48:51], s4 offen\n"
"buffer_load_dwordx4 v[140:143], v96, s[44:47], 0 offen\n"
"buffer_load_dwordx4 v[150:153], v96, s[48:51], 0 offen\n"
"buffer_load_dwordx4 v[130:133], v96, s[40:43], 0 offen\n"
".LPARC:\n"
"s_setprio 0\n"
"s_mov_b32 s4, 0xc0135761\n"
"s_mov_b32 s5, 0xbdd2d3e8\n"
"s_waitcnt vmcnt(0) lgkmcnt(0)\n"
"v_mul_f32 v99, s5, v134\n"
"v_mul_f32 v139, v144, v144\n"
"v_mul_f32 v148, v134, v154\n"
"v_mul_f32 v149, v99, v134\n"
"v_mul_f32 v158, v144, v154\n"
"v_mul_f32 v99, 0x40400000, v99\n"
"v_mul_f32 v108, v149, v134\n"
"v_mul_f32 v128, s4, v134\n"
"v_mul_f32 v149, 0x40400000, v149\n"
"v_fmac_f32 v128, v99, v139\n"
"v_mul_f32 v118, v149, v144\n"
"v_mov_b32 v99, s4\n"
"v_fmac_f32 v99, s5, v139\n"
"v_mul_f32 v138, v144, v99\n"
"v_and_b32 v96, 0xffff0000, v133\n"
"v_and_b32 v97, 0xffff0000, v143\n"
"v_and_b32 v98, 0xffff0000, v153\n"
"v_mul_f32 v99, s5, v96\n"
"v_mul_f32 v139, v97, v97\n"
"v_mul_f32 v147, v96, v98\n"
"v_mul_f32 v149, v99, v96\n"
"v_mul_f32 v157, v97, v98\n"
"v_mul_f32 v99, 0x40400000, v99\n"
"v_mul_f32 v107, v149, v96\n"
"v_mul_f32 v127, s4, v96\n"
"v_mul_f32 v149, 0x40400000, v149\n"
"v_fmac_f32 v127, v99, v139\n"
"v_mul_f32 v117, v149, v97\n"
"v_mov_b32 v99, s4\n"
"v_fmac_f32 v99, s5, v139\n"
"v_mul_f32 v137, v97, v99\n"
"v_lshlrev_b32 v96, 16, v133\n"
"v_lshlrev_b32 v97, 16, v143\n"
"v_lshlrev_b32 v98, 16, v153\n"
"v_mul_f32 v99, s5, v96\n"
"v_mul_f32 v139, v97, v97\n"
"v_mul_f32 v146, v96, v98\n"
"v_mul_f32 v149, v99, v96\n"
"v_mul_f32 v156, v97, v98\n"
"v_mul_f32 v99, 0x40400000, v99\n"
"v_mul_f32 v106, v149, v96\n"
"v_mul_f32 v126, s4, v96\n"
"v_mul_f32 v149, 0x40400000, v149\n"
"v_fmac_f32 v126, v99, v139\n"
"v_mul_f32 v116, v149, v97\n"
"v_mov_b32 v99, s4\n"
"v_fmac_f32 v99, s5, v139\n"
"v_mul_f32 v136, v97, v99\n"
"v_and_b32 v96, 0xffff0000, v132\n"
"v_and_b32 v97, 0xffff0000, v142\n"
"v_and_b32 v98, 0xffff0000, v152\n"
"v_mul_f32 v99, s5, v96\n"
"v_mul_f32 v139, v97, v97\n"
"v_mul_f32 v145, v96, v98\n"
"v_mul_f32 v149, v99, v96\n"
"v_mul_f32 v155, v97, v98\n"
"v_mul_f32 v99, 0x40400000, v99\n"
"v_mul_f32 v105, v149, v96\n"
"v_mul_f32 v125, s4, v96\n"
"v_mul_f32 v149, 0x40400000, v149\n"
"v_fmac_f32 v125, v99, v139\n"
"v_mul_f32 v115, v149, v97\n"
"v_mov_b32 v99, s4\n"
"v_fmac_f32 v99, s5, v139\n"
"v_mul_f32 v135, v97, v99\n"
"v_lshlrev_b32 v96, 16, v132\n"
"v_lshlrev_b32 v97, 16, v142\n"
"v_lshlrev_b32 v98, 16, v152\n"
"v_mul_f32 v99, s5, v96\n"
"v_mul_f32 v139, v97, v97\n"
"v_mul_f32 v144, v96, v98\n"
"v_mul_f32 v149, v99, v96\n"
"v_mul_f32 v154, v97, v98\n"
"v_mul_f32 v99, 0x40400000, v99\n"
"v_mul_f32 v104, v149, v96\n"
"v_mul_f32 v124, s4, v96\n"
"v_mul_f32 v149, 0x40400000, v149\n"
"v_fmac_f32 v124, v99, v139\n"
"v_mul_f32 v114, v149, v97\n"
"v_mov_b32 v99, s4\n"
"v_fmac_f32 v99, s5, v139\n"
"v_mul_f32 v134, v97, v99\n"
"v_and_b32 v96, 0xffff0000, v131\n"
"v_and_b32 v97, 0xffff0000, v141\n"
"v_and_b32 v98, 0xffff0000, v151\n"
"v_mul_f32 v99, s5, v96\n"
"v_mul_f32 v139, v97, v97\n"
"v_mul_f32 v143, v96, v98\n"
"v_mul_f32 v149, v99, v96\n"
"v_mul_f32 v153, v97, v98\n"
"v_mul_f32 v99, 0x40400000, v99\n"
"v_mul_f32 v103, v149, v96\n"
"v_mul_f32 v123, s4, v96\n"
"v_mul_f32 v149, 0x40400000, v149\n"
"v_fmac_f32 v123, v99, v139\n"
"v_mul_f32 v113, v149, v97\n"
"v_mov_b32 v99, s4\n"
"v_fmac_f32 v99, s5, v139\n"
"v_mul_f32 v133, v97, v99\n"
"v_lshlrev_b32 v96, 16, v131\n"
"v_lshlrev_b32 v97, 16, v141\n"
"v_lshlrev_b32 v98, 16, v151\n"
"v_mul_f32 v99, s5, v96\n"
"v_mul_f32 v139, v97, v97\n"
"v_mul_f32 v142, v96, v98\n"
"v_mul_f32 v149, v99, v96\n"
"v_mul_f32 v152, v97, v98\n"
"v_mul_f32 v99, 0x40400000, v99\n"
"v_mul_f32 v102, v149, v96\n"
"v_mul_f32 v122, s4, v96\n"
"v_mul_f32 v149, 0x40400000, v149\n"
"v_fmac_f32 v122, v99, v139\n"
"v_mul_f32 v112, v149, v97\n"
"v_mov_b32 v99, s4\n"
"v_fmac_f32 v99, s5, v139\n"
"v_mul_f32 v132, v97, v99\n"
"v_and_b32 v96, 0xffff0000, v130\n"
"v_and_b32 v97, 0xffff0000, v140\n"
"v_and_b32 v98, 0xffff0000, v150\n"
"v_mul_f32 v99, s5, v96\n"
"v_mul_f32 v139, v97, v97\n"
"v_mul_f32 v141, v96, v98\n"
"v_mul_f32 v149, v99, v96\n"
"v_mul_f32 v151, v97, v98\n"
"v_mul_f32 v99, 0x40400000, v99\n"
"v_mul_f32 v101, v149, v96\n"
"v_mul_f32 v121, s4, v96\n"
"v_mul_f32 v149, 0x40400000, v149\n"
"v_fmac_f32 v121, v99, v139\n"
"v_mul_f32 v111, v149, v97\n"
"v_mov_b32 v99, s4\n"
"v_fmac_f32 v99, s5, v139\n"
"v_mul_f32 v131, v97, v99\n"
"v_lshlrev_b32 v96, 16, v130\n"
"v_lshlrev_b32 v97, 16, v140\n"
"v_lshlrev_b32 v98, 16, v150\n"
"v_mul_f32 v99, s5, v96\n"
"s_cbranch_execz .LRET\n"
"v_mul_f32 v139, v97, v97\n"
"v_mul_f32 v140, v96, v98\n"
"v_mul_f32 v149, v99, v96\n"
"v_mul_f32 v150, v97, v98\n"
"v_mul_f32 v99, 0x40400000, v99\n"
"v_mul_f32 v100, v149, v96\n"
"v_mul_f32 v120, s4, v96\n"
"v_mul_f32 v149, 0x40400000, v149\n"
"v_fmac_f32 v120, v99, v139\n"
"v_mul_f32 v110, v149, v97\n"
"v_mov_b32 v99, s4\n"
"v_fmac_f32 v99, s5, v139\n"
"v_mul_f32 v130, v97, v99\n"
"s_setpc_b64 s[0:1]\n"
".LMOD:\n"
"s_mov_b32 s4, 296960\n"
"s_mov_b32 s5, 301568\n"
".LP_m:\n"
"buffer_load_dwordx4 v[32:35], v2, s[16:19], s4 offen offset:0 sc1\n"
"buffer_load_dwordx4 v[36:39], v2, s[16:19], s4 offen offset:1024 sc1\n"
"buffer_load_dwordx4 v[44:47], v2, s[16:19], s4 offen offset:2304 sc1\n"
"buffer_load_dwordx4 v[48:51], v2, s[16:19], s4 offen offset:3328 sc1\n"
"buffer_load_dwordx4 v[56:59], v2, s[16:19], s5 offen offset:0 sc1\n"
"buffer_load_dwordx4 v[60:63], v2, s[16:19], s5 offen offset:1024 sc1\n"
"buffer_load_dwordx4 v[68:71], v2, s[16:19], s5 offen offset:2304 sc1\n"
"buffer_load_dwordx4 v[72:75], v2, s[16:19], s5 offen offset:3328 sc1\n"
"s_mov_b64 exec, 0xffff\n"
"s_add_u32 s6, s4, 2048\n"
"s_add_u32 s7, s5, 2048\n"
"buffer_load_dwordx4 v[40:43], v2, s[16:19], s6 offen offset:0 sc1\n"
"buffer_load_dwordx4 v[52:55], v2, s[16:19], s6 offen offset:2304 sc1\n"
"buffer_load_dwordx4 v[64:67], v2, s[16:19], s7 offen offset:0 sc1\n"
"buffer_load_dwordx4 v[76:79], v2, s[16:19], s7 offen offset:2304 sc1\n"
"s_waitcnt vmcnt(0)\n"
"v_max3_u32 v21, v40, v41, v42\n"
"v_max3_u32 v21, v21, v43, v52\n"
"v_max3_u32 v21, v21, v53, v54\n"
"v_max3_u32 v21, v21, v55, v64\n"
"v_max3_u32 v21, v21, v65, v66\n"
"v_max3_u32 v21, v21, v67, v76\n"
"v_max3_u32 v21, v21, v77, v78\n"
"v_max_u32 v21, v21, v79\n"
"v_cmp_eq_u32 s[6:7], -1, v21\n"
"s_mov_b64 exec, -1\n"
"v_max3_u32 v20, v32, v33, v34\n"
"v_max3_u32 v20, v20, v35, v36\n"
"v_max3_u32 v20, v20, v37, v38\n"
"v_max3_u32 v20, v20, v39, v44\n"
"v_max3_u32 v20, v20, v45, v46\n"
"v_max3_u32 v20, v20, v47, v48\n"
"v_max3_u32 v20, v20, v49, v50\n"
"v_max3_u32 v20, v20, v51, v56\n"
"v_max3_u32 v20, v20, v57, v58\n"
"v_max3_u32 v20, v20, v59, v60\n"
"v_max3_u32 v20, v20, v61, v62\n"
"v_max3_u32 v20, v20, v63, v68\n"
"v_max3_u32 v20, v20, v69, v70\n"
"v_max3_u32 v20, v20, v71, v72\n"
"v_max3_u32 v20, v20, v73, v74\n"
"v_max_u32 v20, v20, v75\n"
"v_cmp_eq_u32 vcc, -1, v20\n"
"s_or_b64 vcc, vcc, s[6:7]\n"
"s_cbranch_vccnz .LR_m\n"
".LD_m:\n"
"v_mov_b64 v[80:81], 0\n"
"v_mov_b64 v[82:83], 0\n"
"v_mov_b64 v[84:85], 0\n"
"v_mov_b64 v[86:87], 0\n"
"v_mov_b64 v[88:89], 0\n"
"v_pk_add_f32 v[56:57], v[56:57], 1.0 op_sel_hi:[1,0]\n"
"v_pk_add_f32 v[58:59], v[58:59], 1.0 op_sel_hi:[1,0]\n"
"v_pk_add_f32 v[60:61], v[60:61], 1.0 op_sel_hi:[1,0]\n"
"v_pk_add_f32 v[62:63], v[62:63], 1.0 op_sel_hi:[1,0]\n"
"v_pk_add_f32 v[64:65], v[64:65], 1.0 op_sel_hi:[1,0]\n"
"v_pk_add_f32 v[66:67], v[66:67], 1.0 op_sel_hi:[1,0]\n"
"v_cvt_pk_bf16_f32 v57, v56, v57\n"
"v_cvt_pk_bf16_f32 v59, v58, v59\n"
"v_cvt_pk_bf16_f32 v61, v60, v61\n"
"v_cvt_pk_bf16_f32 v63, v62, v63\n"
"v_cvt_pk_bf16_f32 v65, v64, v65\n"
"v_cvt_pk_bf16_f32 v67, v66, v67\n"
"v_lshlrev_b32 v56, 16, v57\n"
"v_and_b32 v57, 0xffff0000, v57\n"
"v_lshlrev_b32 v58, 16, v59\n"
"v_and_b32 v59, 0xffff0000, v59\n"
"v_lshlrev_b32 v60, 16, v61\n"
"v_and_b32 v61, 0xffff0000, v61\n"
"v_lshlrev_b32 v62, 16, v63\n"
"v_and_b32 v63, 0xffff0000, v63\n"
"v_lshlrev_b32 v64, 16, v65\n"
"v_and_b32 v65, 0xffff0000, v65\n"
"v_lshlrev_b32 v66, 16, v67\n"
"v_and_b32 v67, 0xffff0000, v67\n"
"v_pk_add_f32 v[86:87], v[86:87], v[44:45]\n"
"v_pk_fma_f32 v[88:89], v[44:45], v[44:45], v[88:89]\n"
"v_pk_add_f32 v[80:81], v[80:81], v[56:57]\n"
"v_pk_fma_f32 v[82:83], v[56:57], v[56:57], v[82:83]\n"
"v_pk_fma_f32 v[84:85], v[56:57], v[44:45], v[84:85]\n"
"v_pk_add_f32 v[86:87], v[86:87], v[46:47]\n"
"v_pk_fma_f32 v[88:89], v[46:47], v[46:47], v[88:89]\n"
"v_pk_add_f32 v[80:81], v[80:81], v[58:59]\n"
"v_pk_fma_f32 v[82:83], v[58:59], v[58:59], v[82:83]\n"
"v_pk_fma_f32 v[84:85], v[58:59], v[46:47], v[84:85]\n"
"v_pk_add_f32 v[86:87], v[86:87], v[48:49]\n"
"v_pk_fma_f32 v[88:89], v[48:49], v[48:49], v[88:89]\n"
"v_pk_add_f32 v[80:81], v[80:81], v[60:61]\n"
"v_pk_fma_f32 v[82:83], v[60:61], v[60:61], v[82:83]\n"
"v_pk_fma_f32 v[84:85], v[60:61], v[48:49], v[84:85]\n"
"v_pk_add_f32 v[86:87], v[86:87], v[50:51]\n"
"v_pk_fma_f32 v[88:89], v[50:51], v[50:51], v[88:89]\n"
"v_pk_add_f32 v[80:81], v[80:81], v[62:63]\n"
"v_pk_fma_f32 v[82:83], v[62:63], v[62:63], v[82:83]\n"
"v_pk_fma_f32 v[84:85], v[62:63], v[50:51], v[84:85]\n"
"s_mov_b64 exec, 0xffff\n"
"v_pk_add_f32 v[86:87], v[86:87], v[52:53]\n"
"v_pk_fma_f32 v[88:89], v[52:53], v[52:53], v[88:89]\n"
"v_pk_add_f32 v[80:81], v[80:81], v[64:65]\n"
"v_pk_fma_f32 v[82:83], v[64:65], v[64:65], v[82:83]\n"
"v_pk_fma_f32 v[84:85], v[64:65], v[52:53], v[84:85]\n"
"v_pk_add_f32 v[86:87], v[86:87], v[54:55]\n"
"v_pk_fma_f32 v[88:89], v[54:55], v[54:55], v[88:89]\n"
"v_pk_add_f32 v[80:81], v[80:81], v[66:67]\n"
"v_pk_fma_f32 v[82:83], v[66:67], v[66:67], v[82:83]\n"
"v_pk_fma_f32 v[84:85], v[66:67], v[54:55], v[84:85]\n"
"ds_write_b128 v2, v[40:43] offset:8400\n"
"ds_write_b128 v2, v[52:55] offset:10704\n"
"ds_write_b128 v2, v[64:67] offset:13008\n"
"ds_write_b128 v2, v[76:79] offset:15312\n"
"s_mov_b64 exec, -1\n"
"ds_write_b128 v2, v[32:35] offset:6352\n"
"ds_write_b128 v2, v[36:39] offset:7376\n"
"ds_write_b128 v2, v[44:47] offset:8656\n"
"ds_write_b128 v2, v[48:51] offset:9680\n"
"ds_write_b128 v2, v[56:59] offset:10960\n"
"ds_write_b128 v2, v[60:63] offset:11984\n"
"ds_write_b128 v2, v[68:71] offset:13264\n"
"ds_write_b128 v2, v[72:75] offset:14288\n"
"v_add_f32 v20, v80, v81\n"
"v_add_f32 v21, v82, v83\n"
"v_add_f32 v22, v84, v85\n"
"v_add_f32 v23, v86, v87\n"
"v_add_f32 v24, v88, v89\n"
"v_add_f32 v22, v22, v22\n"
"v_mov_b32 v25, v24\n"
"v_permlane32_swap_b32 v20, v21\n"
"v_permlane32_swap_b32 v22, v23\n"
"v_permlane32_swap_b32 v24, v25\n"
"v_add_f32 v20, v20, v21\n"
"v_add_f32 v22, v22, v23\n"
"v_add_f32 v24, v24, v25\n"
"v_mov_b32 v25, v24\n"
"v_permlane16_swap_b32 v20, v22\n"
"v_add_f32 v20, v20, v22\n"
"v_permlane16_swap_b32 v24, v25\n"
"v_add_f32 v24, v24, v25\n"
"s_cbranch_execz .LRET\n"
"v_add_f32_dpp v20, v20, v20 quad_perm:[1,0,3,2] row_mask:0xf bank_mask:0xf\n"
"s_nop 0\n"
"v_add_f32_dpp v24, v24, v24 quad_perm:[1,0,3,2] row_mask:0xf bank_mask:0xf\n"
"v_add_f32_dpp v20, v20, v20 quad_perm:[2,3,0,1] row_mask:0xf bank_mask:0xf\n"
"s_nop 0\n"
"v_add_f32_dpp v24, v24, v24 quad_perm:[2,3,0,1] row_mask:0xf bank_mask:0xf\n"
"v_add_f32_dpp v20, v20, v20 row_half_mirror row_mask:0xf bank_mask:0xf\n"
"s_nop 0\n"
"v_add_f32_dpp v24, v24, v24 row_half_mirror row_mask:0xf bank_mask:0xf\n"
"v_add_f32_dpp v20, v20, v20 row_ror:8 row_mask:0xf bank_mask:0xf\n"
"s_nop 0\n"
"v_add_f32_dpp v24, v24, v24 row_ror:8 row_mask:0xf bank_mask:0xf\n"
"v_lshrrev_b32 v4, 5, v1\n"
"v_bfe_u32 v5, v1, 4, 1\n"
"v_lshl_or_b32 v6, v5, 1, v4\n"
"v_lshlrev_b32 v6, 2, v6\n"
"v_mov_b32 v4, 0\n"
"v_mul_f32 v20, 0x3ae38e39, v20\n"
"v_mov_b32 v25, 0x3727c5ac\n"
"v_fmac_f32 v25, 0x3ae38e39, v24\n"
"s_mov_b32 exec_lo, 0x10001\n"
"s_mov_b32 exec_hi, 0x10001\n"
"ds_write_b32 v6, v20 offset:6320\n"
"ds_write_b32 v4, v25 offset:6336\n"
"s_mov_b64 exec, -1\n"
"s_setpc_b64 s[0:1]\n"
".LS5A:\n"
"s_cmp_eq_u32 s3, 8\n"
"s_cbranch_scc1 .LS5M\n"
"s_getpc_b64 s[0:1]\n"
".LS5Pq:\n"
"s_add_u32 s0, s0, .LS5P-.LS5Pq\n"
"s_addc_u32 s1, s1, 0\n"
"s_branch .LPAR\n"
".LS5P:\n"
"s_branch .LS5B\n"
".LS5M:\n"
"s_getpc_b64 s[0:1]\n"
".LS5Bq:\n"
"s_add_u32 s0, s0, .LS5B-.LS5Bq\n"
"s_addc_u32 s1, s1, 0\n"
"s_branch .LMOD\n"
".LS5B:\n"
"v_and_b32 v3, 15, v1\n"
"v_lshrrev_b32 v4, 5, v1\n"
"v_bfe_u32 v5, v1, 4, 1\n"
"v_lshl_or_b32 v6, v5, 1, v4\n"
"v_and_b32 v7, 7, v1\n"
"v_lshlrev_b32 v7, 3, v7\n"
"v_lshl_or_b32 v7, v6, 1, v7\n"
"v_bfe_u32 v8, v1, 3, 1\n"
"v_or_b32 v7, v7, v8\n"
"s_lshl_b32 s4, s3, 6\n"
"v_add_u32 v7, s4, v7\n"
"v_and_b32 v8, 3, v1\n"
"v_min_u32 v8, 2, v8\n"
"s_mul_i32 s5, s65, 3\n"
"v_add_u32 v8, s5, v8\n"
"v_mov_b32 v10, 0\n"
"v_mov_b32 v11, 0\n"
"v_lshlrev_b32 v9, 1, v7\n"
"v_and_b32 v14, 0xffffffd7, v7\n"
"v_and_b32 v16, 32, v7\n"
"v_lshrrev_b32 v16, 2, v16\n"
"v_or_b32 v14, v14, v16\n"
"v_and_b32 v16, 8, v7\n"
"v_lshl_or_b32 v14, v16, 2, v14\n"
"v_lshlrev_b32 v14, 2, v14\n"
"v_and_b32 v15, 0xffffffd7, v8\n"
"v_and_b32 v16, 32, v8\n"
"v_lshrrev_b32 v16, 2, v16\n"
"v_or_b32 v15, v15, v16\n"
"v_and_b32 v16, 8, v8\n"
"v_lshl_or_b32 v15, v16, 2, v15\n"
"v_lshlrev_b32 v15, 2, v15\n"
"s_lshl_b32 s4, s3, 5\n"
"v_lshl_add_u32 v16, v6, 2, s4\n"
"v_lshl_add_u32 v17, v4, 2, s4\n"
"v_lshlrev_b32 v18, 2, v1\n"
"v_and_b32 v19, 7, v1\n"
"v_lshlrev_b32 v19, 2, v19\n"
"v_mov_b32 v20, 0\n"
"buffer_load_short_d16_hi v10, v9, s[32:35], 0 offen\n"
"buffer_load_short_d16_hi v11, off, s[52:55], 0\n"
"buffer_load_dwordx2 v[12:13], off, s[16:19], 0 sc1\n"
"s_waitcnt lgkmcnt(0)\n"
"s_barrier\n"
"ds_read_b32 v32, v14 offset:6352\n"
"ds_read_b32 v33, v14 offset:8656\n"
"ds_read_b32 v34, v14 offset:10960\n"
"ds_read_b32 v35, v15 offset:8656\n"
"ds_read_b32 v36, v15 offset:10960\n"
"ds_read_b32 v37, v15 offset:13264\n"
"ds_read_b128 v[40:43], v20 offset:6320\n"
"ds_read_b32 v44, v20 offset:6336\n"
"v_mov_b32 v45, 0x3727c5ac\n"
"v_lshrrev_b32 v14, 3, v1\n"
"v_mul_u32_u24 v14, 1152, v14\n"
"v_and_b32 v3, 7, v1\n"
"v_lshl_add_u32 v14, v3, 4, v14\n"
"s_lshl_b32 s4, s3, 7\n"
"s_add_u32 s4, s4, 278528\n"
"s_add_u32 s5, s4, 9216\n"
"v_lshlrev_b32 v15, 2, v7\n"
"v_lshlrev_b32 v9, 2, v8\n"
"s_lshl_b32 s6, s3, 4\n"
"v_lshl_add_u32 v46, v6, 2, s6\n"
"v_and_b32 v47, 31, v1\n"
"v_lshlrev_b32 v47, 2, v47\n"
"v_lshlrev_b32 v8, 1, v8\n"
"s_waitcnt lgkmcnt(0)\n"
"v_add_f32 v38, v33, v33\n"
"v_mul_f32 v39, -2.0, v34\n"
"s_setprio 2\n"
"buffer_load_dwordx4 v[48:51], v14, s[16:19], s4 offen sc1\n"
"s_waitcnt vmcnt(0)\n"
"v_max3_u32 v20, v48, v49, v50\n"
"v_max_u32 v20, v20, v51\n"
"v_cmp_eq_u32 vcc, -1, v20\n"
"s_cbranch_vccnz .LR_x\n"
".LD_x:\n"
"v_permlane32_swap_b32 v48, v49\n"
"v_permlane32_swap_b32 v50, v51\n"
"v_lshlrev_b32 v52, 16, v48\n"
"v_lshlrev_b32 v53, 16, v49\n"
"v_lshlrev_b32 v54, 16, v50\n"
"v_lshlrev_b32 v55, 16, v51\n"
"v_and_b32 v48, 0xffff0000, v48\n"
"v_and_b32 v49, 0xffff0000, v49\n"
"v_and_b32 v50, 0xffff0000, v50\n"
"v_and_b32 v51, 0xffff0000, v51\n"
"v_add_f32 v52, v52, v53\n"
"v_add_f32 v54, v54, v55\n"
"v_add_f32 v48, v48, v49\n"
"v_add_f32 v50, v50, v51\n"
"v_permlane16_swap_b32 v52, v54\n"
"v_add_f32 v52, v52, v54\n"
"v_permlane16_swap_b32 v48, v50\n"
"v_add_f32 v49, v48, v50\n"
"v_add_f32_dpp v48, v52, v52 row_ror:8 row_mask:0xf bank_mask:0x3\n"
"s_nop 0\n"
"v_add_f32_dpp v48, v49, v49 row_ror:8 row_mask:0xf bank_mask:0xc\n"
"v_cvt_pk_bf16_f32 v48, v48, v48\n"
"v_lshlrev_b32 v48, 16, v48\n"
"v_mul_f32 v48, v32, v48\n"
"v_cvt_pk_bf16_f32 v48, v48, v48\n"
"v_lshlrev_b32 v48, 16, v48\n"
"v_add_f32 v48, v10, v48\n"
"v_cvt_pk_bf16_f32 v20, v48, v48\n"
"v_lshlrev_b32 v20, 16, v20\n"
"v_mul_f32 v22, v20, v34\n"
"ds_write_b32 v15, v20 offset:15568\n"
"v_mul_f32 v21, v20, v20\n"
"v_mul_f32 v23, v22, v22\n"
"v_mul_f32 v24, v22, v39\n"
"v_mul_f32 v25, v22, v38\n"
"v_permlane32_swap_b32 v20, v21\n"
"v_permlane32_swap_b32 v22, v23\n"
"v_permlane32_swap_b32 v24, v25\n"
"v_add_f32 v20, v20, v21\n"
"v_add_f32 v22, v22, v23\n"
"v_add_f32 v24, v24, v25\n"
"v_mov_b32 v25, v24\n"
"v_permlane16_swap_b32 v20, v22\n"
"v_add_f32 v20, v20, v22\n"
"v_permlane16_swap_b32 v24, v25\n"
"v_add_f32 v24, v24, v25\n"
"v_add_f32_dpp v20, v20, v20 quad_perm:[1,0,3,2] row_mask:0xf bank_mask:0xf\n"
"s_nop 0\n"
"v_add_f32_dpp v24, v24, v24 quad_perm:[1,0,3,2] row_mask:0xf bank_mask:0xf\n"
"v_add_f32_dpp v20, v20, v20 quad_perm:[2,3,0,1] row_mask:0xf bank_mask:0xf\n"
"s_nop 0\n"
"v_add_f32_dpp v24, v24, v24 quad_perm:[2,3,0,1] row_mask:0xf bank_mask:0xf\n"
"v_add_f32_dpp v20, v20, v20 row_half_mirror row_mask:0xf bank_mask:0xf\n"
"s_nop 0\n"
"v_add_f32_dpp v24, v24, v24 row_half_mirror row_mask:0xf bank_mask:0xf\n"
"v_add_f32_dpp v20, v20, v20 row_ror:8 row_mask:0xf bank_mask:0xf\n"
"s_nop 0\n"
"v_add_f32_dpp v24, v24, v24 row_ror:8 row_mask:0xf bank_mask:0xf\n"
"s_mov_b32 exec_lo, 0x10001\n"
"s_mov_b32 exec_hi, 0x10001\n"
"ds_write_b32 v16, v20 offset:6032\n"
"ds_write_b32 v17, v24 offset:6048\n"
"s_mov_b64 exec, -1\n"
"s_waitcnt lgkmcnt(0)\n"
"s_barrier\n"
"s_cmp_eq_u32 s3, 8\n"
"s_cbranch_scc1 .LEND\n"
"ds_read_b32 v20, v18 offset:6032\n"
"ds_read_b32 v21, v19 offset:6288\n"
"s_cbranch_execz .LRET\n"
"ds_read_b32 v31, v9 offset:15568\n"
"s_waitcnt lgkmcnt(2)\n"
"v_add_f32_dpp v20, v20, v20 row_ror:8 row_mask:0xf bank_mask:0xf\n"
"v_mov_b32 v22, v20\n"
"s_nop 1\n"
"v_permlane16_swap_b32 v20, v22\n"
"v_add_f32 v20, v20, v22\n"
"v_mov_b32 v22, v20\n"
"s_nop 1\n"
"v_permlane32_swap_b32 v20, v22\n"
"v_add_f32 v20, v20, v22\n"
"s_waitcnt lgkmcnt(0)\n"
"v_add_f32 v20, v20, v21\n"
"v_mul_f32 v20, 0x3ae38e39, v20\n"
"s_nop 1\n"
"v_mov_b32_dpp v21, v20 row_newbcast:0 row_mask:0xf bank_mask:0xf\n"
"v_add_f32_dpp v22, v20, v45 row_newbcast:1 row_mask:0xf bank_mask:0xf\n"
"v_mov_b32_dpp v23, v20 row_newbcast:2 row_mask:0xf bank_mask:0xf\n"
"v_fma_f32 v22, -v21, v21, v22\n"
"v_mov_b32_dpp v25, v20 row_newbcast:4 row_mask:0xf bank_mask:0xf\n"
"v_max_f32 v22, v22, v45\n"
"v_mov_b32_dpp v24, v20 row_newbcast:3 row_mask:0xf bank_mask:0xf\n"
"v_rsq_f32 v22, v22\n"
"v_mov_b32_dpp v26, v20 row_newbcast:5 row_mask:0xf bank_mask:0xf\n"
"v_fmac_f32 v25, v21, v41\n"
"v_fma_f32 v23, -v21, v40, v23\n"
"v_fmac_f32 v24, v21, v25\n"
"v_fma_f32 v26, -v21, v42, v26\n"
"v_sub_f32 v27, v31, v21\n"
"v_fmac_f32 v26, v24, v22\n"
"v_fma_f32 v23, v23, v22, v43\n"
"v_fma_f32 v26, v26, v22, v44\n"
"v_mul_f32 v27, v27, v36\n"
"v_fma_f32 v26, -v23, v23, v26\n"
"v_fma_f32 v27, v27, v22, v35\n"
"v_max_f32 v26, v26, v45\n"
"v_rsq_f32 v26, v26\n"
"v_sub_f32 v27, v27, v23\n"
"v_mul_f32 v27, v27, v26\n"
"s_nop 1\n"
"v_mov_b32_dpp v28, v27 quad_perm:[0,0,0,0] row_mask:0xf bank_mask:0xf\n"
"v_mov_b32_dpp v29, v27 quad_perm:[1,1,1,1] row_mask:0xf bank_mask:0xf\n"
"v_mov_b32_dpp v30, v27 quad_perm:[2,2,2,2] row_mask:0xf bank_mask:0xf\n"
"s_setprio 0\n"
"v_pk_fma_f32 v[48:49], v[100:101], v[28:29], v[110:111] op_sel_hi:[1,0,1]\n"
"v_pk_fma_f32 v[50:51], v[100:101], v[28:29], v[110:111] op_sel:[0,1,0] op_sel_hi:[1,1,1]\n"
"v_pk_fma_f32 v[52:53], v[100:101], v[30:31], v[110:111] op_sel_hi:[1,0,1]\n"
"v_pk_fma_f32 v[48:49], v[48:49], v[28:29], v[120:121] op_sel_hi:[1,0,1]\n"
"v_pk_fma_f32 v[50:51], v[50:51], v[28:29], v[120:121] op_sel:[0,1,0] op_sel_hi:[1,1,1]\n"
"v_pk_fma_f32 v[52:53], v[52:53], v[30:31], v[120:121] op_sel_hi:[1,0,1]\n"
"v_pk_fma_f32 v[48:49], v[48:49], v[28:29], v[130:131] op_sel_hi:[1,0,1]\n"
"v_pk_fma_f32 v[50:51], v[50:51], v[28:29], v[130:131] op_sel:[0,1,0] op_sel_hi:[1,1,1]\n"
"v_pk_fma_f32 v[52:53], v[52:53], v[30:31], v[130:131] op_sel_hi:[1,0,1]\n"
"v_exp_f32 v48, v48\n"
"v_exp_f32 v49, v49\n"
"v_exp_f32 v50, v50\n"
"v_exp_f32 v51, v51\n"
"v_exp_f32 v52, v52\n"
"v_exp_f32 v53, v53\n"
"v_pk_fma_f32 v[54:55], v[140:141], v[28:29], v[150:151] op_sel_hi:[1,0,1]\n"
"v_pk_fma_f32 v[56:57], v[140:141], v[28:29], v[150:151] op_sel:[0,1,0] op_sel_hi:[1,1,1]\n"
"v_pk_fma_f32 v[58:59], v[140:141], v[30:31], v[150:151] op_sel_hi:[1,0,1]\n"
"v_pk_add_f32 v[48:49], v[48:49], 1.0 op_sel_hi:[1,0]\n"
"v_pk_add_f32 v[50:51], v[50:51], 1.0 op_sel_hi:[1,0]\n"
"v_pk_add_f32 v[52:53], v[52:53], 1.0 op_sel_hi:[1,0]\n"
"v_rcp_f32 v48, v48\n"
"v_rcp_f32 v49, v49\n"
"v_rcp_f32 v50, v50\n"
"v_rcp_f32 v51, v51\n"
"v_rcp_f32 v52, v52\n"
"v_rcp_f32 v53, v53\n"
"v_pk_mul_f32 v[20:21], v[54:55], v[48:49]\n"
"v_pk_mul_f32 v[22:23], v[56:57], v[50:51]\n"
"v_pk_mul_f32 v[24:25], v[58:59], v[52:53]\n"
"v_pk_fma_f32 v[48:49], v[102:103], v[28:29], v[112:113] op_sel_hi:[1,0,1]\n"
"v_pk_fma_f32 v[50:51], v[102:103], v[28:29], v[112:113] op_sel:[0,1,0] op_sel_hi:[1,1,1]\n"
"v_pk_fma_f32 v[52:53], v[102:103], v[30:31], v[112:113] op_sel_hi:[1,0,1]\n"
"v_pk_fma_f32 v[48:49], v[48:49], v[28:29], v[122:123] op_sel_hi:[1,0,1]\n"
"v_pk_fma_f32 v[50:51], v[50:51], v[28:29], v[122:123] op_sel:[0,1,0] op_sel_hi:[1,1,1]\n"
"v_pk_fma_f32 v[52:53], v[52:53], v[30:31], v[122:123] op_sel_hi:[1,0,1]\n"
"v_pk_fma_f32 v[48:49], v[48:49], v[28:29], v[132:133] op_sel_hi:[1,0,1]\n"
"v_pk_fma_f32 v[50:51], v[50:51], v[28:29], v[132:133] op_sel:[0,1,0] op_sel_hi:[1,1,1]\n"
"v_pk_fma_f32 v[52:53], v[52:53], v[30:31], v[132:133] op_sel_hi:[1,0,1]\n"
"v_exp_f32 v48, v48\n"
"v_exp_f32 v49, v49\n"
"v_exp_f32 v50, v50\n"
"v_exp_f32 v51, v51\n"
"v_exp_f32 v52, v52\n"
"v_exp_f32 v53, v53\n"
"v_pk_fma_f32 v[54:55], v[142:143], v[28:29], v[152:153] op_sel_hi:[1,0,1]\n"
"v_pk_fma_f32 v[56:57], v[142:143], v[28:29], v[152:153] op_sel:[0,1,0] op_sel_hi:[1,1,1]\n"
"v_pk_fma_f32 v[58:59], v[142:143], v[30:31], v[152:153] op_sel_hi:[1,0,1]\n"
"v_pk_add_f32 v[48:49], v[48:49], 1.0 op_sel_hi:[1,0]\n"
"v_pk_add_f32 v[50:51], v[50:51], 1.0 op_sel_hi:[1,0]\n"
"v_pk_add_f32 v[52:53], v[52:53], 1.0 op_sel_hi:[1,0]\n"
"v_rcp_f32 v48, v48\n"
"v_rcp_f32 v49, v49\n"
"v_rcp_f32 v50, v50\n"
"v_rcp_f32 v51, v51\n"
"v_rcp_f32 v52, v52\n"
"v_rcp_f32 v53, v53\n"
"v_pk_fma_f32 v[20:21], v[54:55], v[48:49], v[20:21]\n"
"v_pk_fma_f32 v[22:23], v[56:57], v[50:51], v[22:23]\n"
"v_pk_fma_f32 v[24:25], v[58:59], v[52:53], v[24:25]\n"
"v_pk_fma_f32 v[48:49], v[104:105], v[28:29], v[114:115] op_sel_hi:[1,0,1]\n"
"v_pk_fma_f32 v[50:51], v[104:105], v[28:29], v[114:115] op_sel:[0,1,0] op_sel_hi:[1,1,1]\n"
"v_pk_fma_f32 v[52:53], v[104:105], v[30:31], v[114:115] op_sel_hi:[1,0,1]\n"
"v_pk_fma_f32 v[48:49], v[48:49], v[28:29], v[124:125] op_sel_hi:[1,0,1]\n"
"v_pk_fma_f32 v[50:51], v[50:51], v[28:29], v[124:125] op_sel:[0,1,0] op_sel_hi:[1,1,1]\n"
"v_pk_fma_f32 v[52:53], v[52:53], v[30:31], v[124:125] op_sel_hi:[1,0,1]\n"
"v_pk_fma_f32 v[48:49], v[48:49], v[28:29], v[134:135] op_sel_hi:[1,0,1]\n"
"v_pk_fma_f32 v[50:51], v[50:51], v[28:29], v[134:135] op_sel:[0,1,0] op_sel_hi:[1,1,1]\n"
"v_pk_fma_f32 v[52:53], v[52:53], v[30:31], v[134:135] op_sel_hi:[1,0,1]\n"
"v_exp_f32 v48, v48\n"
"v_exp_f32 v49, v49\n"
"v_exp_f32 v50, v50\n"
"v_exp_f32 v51, v51\n"
"v_exp_f32 v52, v52\n"
"v_exp_f32 v53, v53\n"
"v_pk_fma_f32 v[54:55], v[144:145], v[28:29], v[154:155] op_sel_hi:[1,0,1]\n"
"v_pk_fma_f32 v[56:57], v[144:145], v[28:29], v[154:155] op_sel:[0,1,0] op_sel_hi:[1,1,1]\n"
"v_pk_fma_f32 v[58:59], v[144:145], v[30:31], v[154:155] op_sel_hi:[1,0,1]\n"
"v_pk_add_f32 v[48:49], v[48:49], 1.0 op_sel_hi:[1,0]\n"
"v_pk_add_f32 v[50:51], v[50:51], 1.0 op_sel_hi:[1,0]\n"
"v_pk_add_f32 v[52:53], v[52:53], 1.0 op_sel_hi:[1,0]\n"
"v_rcp_f32 v48, v48\n"
"v_rcp_f32 v49, v49\n"
"v_rcp_f32 v50, v50\n"
"v_rcp_f32 v51, v51\n"
"v_rcp_f32 v52, v52\n"
"v_rcp_f32 v53, v53\n"
"v_pk_fma_f32 v[20:21], v[54:55], v[48:49], v[20:21]\n"
"v_pk_fma_f32 v[22:23], v[56:57], v[50:51], v[22:23]\n"
"v_pk_fma_f32 v[24:25], v[58:59], v[52:53], v[24:25]\n"
"v_pk_fma_f32 v[48:49], v[106:107], v[28:29], v[116:117] op_sel_hi:[1,0,1]\n"
"v_pk_fma_f32 v[50:51], v[106:107], v[28:29], v[116:117] op_sel:[0,1,0] op_sel_hi:[1,1,1]\n"
"v_pk_fma_f32 v[52:53], v[106:107], v[30:31], v[116:117] op_sel_hi:[1,0,1]\n"
"v_pk_fma_f32 v[48:49], v[48:49], v[28:29], v[126:127] op_sel_hi:[1,0,1]\n"
"v_pk_fma_f32 v[50:51], v[50:51], v[28:29], v[126:127] op_sel:[0,1,0] op_sel_hi:[1,1,1]\n"
"v_pk_fma_f32 v[52:53], v[52:53], v[30:31], v[126:127] op_sel_hi:[1,0,1]\n"
"v_pk_fma_f32 v[48:49], v[48:49], v[28:29], v[136:137] op_sel_hi:[1,0,1]\n"
"v_pk_fma_f32 v[50:51], v[50:51], v[28:29], v[136:137] op_sel:[0,1,0] op_sel_hi:[1,1,1]\n"
"v_pk_fma_f32 v[52:53], v[52:53], v[30:31], v[136:137] op_sel_hi:[1,0,1]\n"
"v_exp_f32 v48, v48\n"
"v_exp_f32 v49, v49\n"
"v_exp_f32 v50, v50\n"
"v_exp_f32 v51, v51\n"
"v_exp_f32 v52, v52\n"
"v_exp_f32 v53, v53\n"
"v_pk_fma_f32 v[54:55], v[146:147], v[28:29], v[156:157] op_sel_hi:[1,0,1]\n"
"v_pk_fma_f32 v[56:57], v[146:147], v[28:29], v[156:157] op_sel:[0,1,0] op_sel_hi:[1,1,1]\n"
"v_pk_fma_f32 v[58:59], v[146:147], v[30:31], v[156:157] op_sel_hi:[1,0,1]\n"
"v_pk_add_f32 v[48:49], v[48:49], 1.0 op_sel_hi:[1,0]\n"
"v_pk_add_f32 v[50:51], v[50:51], 1.0 op_sel_hi:[1,0]\n"
"v_pk_add_f32 v[52:53], v[52:53], 1.0 op_sel_hi:[1,0]\n"
"v_rcp_f32 v48, v48\n"
"v_rcp_f32 v49, v49\n"
"v_rcp_f32 v50, v50\n"
"v_rcp_f32 v51, v51\n"
"v_rcp_f32 v52, v52\n"
"v_rcp_f32 v53, v53\n"
"v_pk_fma_f32 v[20:21], v[54:55], v[48:49], v[20:21]\n"
"v_pk_fma_f32 v[22:23], v[56:57], v[50:51], v[22:23]\n"
"v_pk_fma_f32 v[24:25], v[58:59], v[52:53], v[24:25]\n"
"s_cbranch_execz .LRET\n"
"v_pk_fma_f32 v[48:49], v[108:109], v[28:29], v[118:119] op_sel_hi:[0,1,0]\n"
"v_fma_f32 v50, v108, v30, v118\n"
"v_add_f32 v20, v20, v21\n"
"v_pk_fma_f32 v[48:49], v[48:49], v[28:29], v[128:129] op_sel_hi:[1,1,0]\n"
"v_fma_f32 v50, v50, v30, v128\n"
"v_add_f32 v21, v22, v23\n"
"v_pk_fma_f32 v[48:49], v[48:49], v[28:29], v[138:139] op_sel_hi:[1,1,0]\n"
"v_fma_f32 v50, v50, v30, v138\n"
"v_add_f32 v22, v24, v25\n"
"v_exp_f32 v48, v48\n"
"v_exp_f32 v49, v49\n"
"v_exp_f32 v50, v50\n"
"v_pk_fma_f32 v[54:55], v[148:149], v[28:29], v[158:159] op_sel_hi:[0,1,0]\n"
"v_fma_f32 v56, v148, v30, v158\n"
"v_pk_add_f32 v[48:49], v[48:49], 1.0 op_sel_hi:[1,0]\n"
"v_add_f32 v50, 1.0, v50\n"
"v_rcp_f32 v48, v48\n"
"v_rcp_f32 v49, v49\n"
"v_rcp_f32 v50, v50\n"
"v_pk_fma_f32 v[20:21], v[54:55], v[48:49], v[20:21]\n"
"v_fmac_f32 v22, v56, v50\n"
"v_mov_b32 v23, v22\n"
"v_permlane32_swap_b32 v20, v21\n"
"v_add_f32 v20, v20, v21\n"
"v_permlane32_swap_b32 v22, v23\n"
"v_add_f32 v22, v22, v23\n"
"s_nop 1\n"
"v_permlane16_swap_b32 v20, v22\n"
"v_add_f32 v20, v20, v22\n"
"s_nop 1\n"
"v_add_f32_dpp v20, v20, v20 quad_perm:[1,0,3,2] row_mask:0xf bank_mask:0xf\n"
"s_nop 1\n"
"v_add_f32_dpp v20, v20, v20 quad_perm:[2,3,0,1] row_mask:0xf bank_mask:0xf\n"
"s_nop 1\n"
"v_add_f32_dpp v20, v20, v20 row_half_mirror row_mask:0xf bank_mask:0xf\n"
"s_nop 1\n"
"v_add_f32_dpp v20, v20, v20 row_ror:8 row_mask:0xf bank_mask:0xf\n"
"s_mov_b32 exec_lo, 0x10001\n"
"s_mov_b32 exec_hi, 0x10001\n"
"ds_write_b32 v46, v20 offset:17936\n"
"s_mov_b64 exec, -1\n"
"s_waitcnt lgkmcnt(0)\n"
"s_barrier\n"
"s_cmp_lg_u32 s3, 0\n"
"s_cbranch_scc1 .LEND\n"
"ds_read_b32 v20, v47 offset:17936\n"
"v_add_co_u32 v12, vcc, 1, v12\n"
"s_waitcnt lgkmcnt(0)\n"
"v_add_f32_dpp v20, v20, v20 row_ror:4 row_mask:0xf bank_mask:0xf\n"
"v_addc_co_u32 v13, vcc, 0, v13, vcc\n"
"s_nop 0\n"
"v_add_f32_dpp v20, v20, v20 row_ror:8 row_mask:0xf bank_mask:0xf\n"
"v_mov_b32 v21, v20\n"
"s_nop 1\n"
"v_permlane16_swap_b32 v20, v21\n"
"v_add_f32 v20, v20, v21\n"
"v_cvt_pk_bf16_f32 v20, v20, v20\n"
"v_lshlrev_b32 v20, 16, v20\n"
"v_add_f32 v20, v20, v11\n"
"v_cvt_pk_bf16_f32 v20, v20, v20\n"
"v_lshlrev_b32 v20, 16, v20\n"
"v_mul_f32 v20, v37, v20\n"
"v_cvt_pk_bf16_f32 v20, v20, v20\n"
"v_lshlrev_b32 v20, 16, v20\n"
"v_add_f32 v20, v31, v20\n"
"v_cvt_pk_bf16_f32 v20, v20, v20\n"
"s_mov_b64 exec, 7\n"
"buffer_store_short v20, v8, s[56:59], 0 offen\n"
"s_cmp_lg_u32 s2, 128\n"
"s_cbranch_scc1 .LEND0\n"
"s_mov_b64 exec, 1\n"
"buffer_store_dwordx2 v[12:13], off, s[16:19], 0 sc1\n"
".LEND0:\n"
".LEND:\n"
"s_endpgm\n"
".LR_f:\n"
"s_sleep 1\n"
"ds_read_b32 v16, v13 offset:256\n"
"s_waitcnt lgkmcnt(0)\n"
"v_readfirstlane_b32 s1, v16\n"
"s_cmp_lg_u32 s1, 1\n"
"s_cbranch_scc1 .LR_f\n"
"s_branch .LP_f\n"
".LR_q:\n"
"s_sleep 1\n"
"s_sub_u32 s62, s62, 1\n"
"s_cbranch_scc0 .LP_q\n"
"s_mov_b32 s62, 0\n"
"s_branch .LD_q\n"
".LR_t0:\n"
"s_sleep 4\n"
"s_sub_u32 s62, s62, 1\n"
"s_cbranch_scc0 .LP_t0\n"
"s_mov_b32 s62, 0\n"
"s_branch .LD_t0\n"
".LR_h:\n"
"s_sleep 1\n"
"s_sub_u32 s62, s62, 1\n"
"s_cbranch_scc0 .LP_h\n"
"s_mov_b32 s62, 0\n"
"s_branch .LD_h\n"
".LR_o:\n"
"s_sleep 1\n"
"s_sub_u32 s62, s62, 1\n"
"s_cbranch_scc0 .LP_o\n"
"s_mov_b32 s62, 0\n"
"s_branch .LD_o\n"
".LR_t1:\n"
"s_sleep 4\n"
"s_sub_u32 s62, s62, 1\n"
"s_cbranch_scc0 .LP_t1\n"
"s_mov_b32 s62, 0\n"
"s_branch .LD_t1\n"
".LR_t2:\n"
"s_sleep 4\n"
"s_sub_u32 s62, s62, 1\n"
"s_cbranch_scc0 .LP_t2\n"
"s_mov_b32 s62, 0\n"
"s_branch .LD_t2\n"
".LR_m:\n"
"s_sleep 4\n"
"s_sub_u32 s62, s62, 1\n"
"s_cbranch_scc0 .LP_m\n"
"s_mov_b32 s62, 0\n"
"s_branch .LD_m\n"
".LR_x:\n"
"s_mov_b64 exec, vcc\n"
"s_sleep 0\n"
"buffer_load_dwordx4 v[48:51], v14, s[16:19], s4 offen sc1\n"
"s_waitcnt vmcnt(0)\n"
"v_max3_u32 v20, v48, v49, v50\n"
"v_max_u32 v20, v20, v51\n"
"v_cmp_eq_u32 vcc, -1, v20\n"
"s_sub_u32 s62, s62, 1\n"
"s_cbranch_scc1 .LT_x\n"
"s_cbranch_vccnz .LR_x\n"
"s_mov_b64 exec, -1\n"
"s_branch .LD_x\n"
".LT_x:\n"
"s_mov_b32 s62, 0\n"
"s_mov_b64 exec, -1\n"
"s_branch .LD_x\n"
  :: "{s[0:1]}"(__builtin_amdgcn_kernarg_segment_ptr()), "{s2}"(blockIdx.x), "{v0}"(threadIdx.x)
  : "memory", "vcc", "scc", "s71", "v160");
  asm volatile("" :: "v"((__attribute__((address_space(3))) char*)smem));
}
extern "C" void kernel_launch(void* const* d_in, const int* in_sizes, int n_in, void* d_out, int out_size, void* d_ws, size_t ws_size, hipStream_t stream) {
  const u32 F = 0x20000;
  adaln_block_kernel<<<256, 576, 0, stream>>>(d_in[2], 32u << 20, F, d_in[1], 32u << 20, F, d_ws, 4u << 20, F, d_in[3], 576u * 8192u, F,
                                              d_in[11], 576u * 6912u, F, d_in[10], 1152u, F, d_in[0], 1152u, F, d_in[4], 4096u * 1152u, F,
                                              d_in[6], 9216u, F, d_in[7], 9216u, F, d_in[8], 9216u, F, d_in[9], 2u, F, d_out, 1152u, F);
}
```
